```python
import torch

def _softmax(x, axis=-1):
    return torch.softmax(x, dim=axis)
def _gelu(x):
    return torch.nn.functional.gelu(x, approximate="tanh")
def _silu(x):
    return torch.nn.functional.silu(x)
def _sigmoid(x):
    return torch.sigmoid(x)
def _layernorm(x, axis=-1, eps=1e-5):
    m = x.mean(dim=axis, keepdim=True)
    v = x.var(dim=axis, keepdim=True, unbiased=False)
    return (x - m) / torch.sqrt(v + eps)
def _scatter_add(tgt, idx, src, axis=0):
    return tgt.clone().index_add_(axis, idx, src)
def _segment_sum(src, seg, n):
    out = torch.zeros((n,) + src.shape[1:], dtype=src.dtype)
    return out.index_add_(0, seg, src)
def _pad(x, pw):
    flat = tuple(v for pair in reversed(pw) for v in pair)
    return torch.nn.functional.pad(x, flat)

kernel_name = 'gated_mix__softmax_full_mha__dense_activation_mish__prompt'

def setup_inputs():

    _w = (lambda a: (lambda t: t.to(torch.bfloat16) if t.is_floating_point() else t)(torch.as_tensor(a)))
    def _A_setup():
        B, S, D = 1, 2048, 448
        return {
            "x": _w((lambda *s: torch.randn(*s, dtype=torch.bfloat16) * 0.05)(B, S, D)),
            "wqkv": _w((lambda *s: torch.randn(*s, dtype=torch.bfloat16) * 0.8)(D, 3 * D)),
            "wo": _w((lambda *s: torch.randn(*s, dtype=torch.bfloat16) * 0.05)(D, D)),
        }
    _A_in = _A_setup()

    def _B_setup():
        B, S, D, F = 1, 2048, 448, 1792
        return {
            "x": _w((lambda *s: torch.randn(*s, dtype=torch.bfloat16) * 0.05)(B, S, D)),
            "w1": _w((lambda *s: torch.randn(*s, dtype=torch.bfloat16) * 12.8)(D, F) * (D ** -0.5)),
            "b1": _w((lambda *s: torch.randn(*s, dtype=torch.bfloat16) * 0.1)(F)),
            "w2": _w((lambda *s: torch.randn(*s, dtype=torch.bfloat16) * 0.1)(F, D) * (F ** -0.5)),
        }
    _B_in = _B_setup()

    return {
        "A_x": _A_in["x"],
        "A_wqkv": _A_in["wqkv"],
        "A_wo": _A_in["wo"],
        "B_x": _B_in["x"],
        "B_w1": _B_in["w1"],
        "B_b1": _B_in["b1"],
        "B_w2": _B_in["w2"],
        "gate_w": _w((lambda *s: torch.randn(*s, dtype=torch.bfloat16) * 0.05)(448) * 16.0),
    }

def reference(A_x, A_wqkv, A_wo, B_x, B_w1, B_b1, B_w2, gate_w):
    def _A_ref(x, wqkv, wo):
        B, S, D = x.shape
        H = 16; Dh = D // H
        qkv = (lambda x, *d: x.permute(*d))((x @ wqkv).reshape(B, S, 3, H, Dh), 2, 0, 3, 1, 4)
        q, k, v = qkv[0], qkv[1], qkv[2]
        scale = Dh ** -0.5
        scores = torch.einsum("bhqd,bhkd->bhqk", q, k) * scale
        p = _softmax(scores, axis=-1)
        o = torch.einsum("bhqk,bhkd->bhqd", p, v)
        return (lambda x, *d: x.permute(*d))(o, 0, 2, 1, 3).reshape(B, S, D) @ wo

    def _B_ref(x, w1, b1, w2):
        h = torch.einsum("bsd,df->bsf", x, w1) + b1
        sp = torch.log1p(torch.exp(-torch.abs(h))) + torch.where(h > 0, h, torch.zeros_like(h))
        act = h * torch.tanh(sp)
        return torch.einsum("bsf,fd->bsd", act, w2)

    x = A_x
    g = _sigmoid((x * gate_w).sum(-1, keepdims=True))
    a = _A_ref(x, A_wqkv, A_wo)
    b = _B_ref(x, B_w1, B_b1, B_w2)
    return g * a + (1 - g) * b
```

```cpp
#include <hip/hip_runtime.h>

struct Buf { const void* p; unsigned bytes, fmt; };
struct Args { Buf x, wqkv, w1, b1, ws, w2, wo, gw, out; };
static_assert(sizeof(Args) == 144, "no padding");
__global__ void __launch_bounds__(512, 2) gm_fused(Args) {
#if defined(__HIP_DEVICE_COMPILE__)
    __shared__ __attribute__((aligned(1024))) char smem[163840];
    auto ka = __builtin_amdgcn_kernarg_segment_ptr();
    unsigned bid = __builtin_amdgcn_workgroup_id_x(), tid = __builtin_amdgcn_workitem_id_x();
    asm volatile(
    "s_load_dwordx16 s[8:23], s[0:1], 0x0\n"
    "s_load_dwordx16 s[24:39], s[0:1], 0x40\n"
    "s_load_dwordx4 s[40:43], s[0:1], 0x80\n"
    "s_getpc_b64 s[80:81]\n"
    ".Lgm_start_2:\n"
    "v_lshrrev_b32 v1, 6, v0\n"
    "v_and_b32 v255, 63, v0\n"
    "v_readfirstlane_b32 s44, v1\n"
    "v_and_b32 v1, 3, v1\n"
    "s_and_b32 s45, s2, 7\n"
    "s_lshr_b32 s46, s2, 3\n"
    "s_cmp_lt_u32 s44, 3\n"
    "s_cbranch_scc0 .Lgm_tp_6\n"
    "v_lshlrev_b32 v4, 7, v255\n"
    "s_cmp_eq_u32 s44, 0\n"
    "s_cbranch_scc0 .Lgm_tp_4\n"
    "s_mul_i32 s84, s46, 1280\n"
    "v_add_u32 v4, s84, v4\n"
    "v_min_u32 v4, .Lgm_end_3-.Lgm_start_2-4, v4\n"
    "s_mov_b64 exec, 0x3ff\n"
    "global_load_dword v111, v4, s[80:81]\n"
    "s_branch .Lgm_tp_5\n"
    ".Lgm_tp_4:\n"
    ".Lgm_tp_5:\n"
    "s_mov_b64 exec, -1\n"
    ".Lgm_tp_6:\n"
    "s_lshr_b32 s84, s45, 1\n"
    "s_lshl_b32 s84, s84, 2\n"
    "s_and_b32 s85, s46, 3\n"
    "s_add_u32 s47, s84, s85\n"
    "s_lshr_b32 s86, s46, 2\n"
    "s_and_b32 s84, s45, 1\n"
    "s_mul_i32 s48, s84, 7\n"
    "s_add_u32 s48, s48, s86\n"
    "s_add_u32 s84, s84, 14\n"
    "s_cmp_ge_u32 s46, 28\n"
    "s_cselect_b32 s48, s84, s48\n"
    "s_lshl_b32 s85, s45, 2\n"
    "s_lshr_b32 s54, 0x26045230, s85\n"
    "s_and_b32 s54, s54, 7\n"
    "s_and_b32 s85, s46, 3\n"
    "s_add_u32 s85, s85, s86\n"
    "s_mul_i32 s85, s85, 3\n"
    "s_lshr_b32 s85, 0x10410410, s85\n"
    "s_and_b32 s85, s85, 7\n"
    "s_add_u32 s54, s54, s85\n"
    "s_cmp_ge_u32 s54, 7\n"
    "s_cselect_b32 s85, 7, 0\n"
    "s_sub_u32 s54, s54, s85\n"
    "s_mul_i32 s62, s48, 3\n"
    "s_lshr_b32 s50, s62, 1\n"
    "s_and_b32 s60, s48, 1\n"
    "s_and_b32 s59, s44, 3\n"
    "s_cmp_lt_u32 s44, 4\n"
    "s_cbranch_scc1 .Lgm_roleA_1\n"
    "s_setprio 1\n"
    "s_cmp_ge_u32 s46, 28\n"
    "s_cbranch_scc1 .Lgm_b_idle_7\n"
    "s_lshl_b32 s53, s59, 10\n"
    "s_mul_i32 s94, s47, 114688\n"
    "s_add_u32 s95, s94, 28672\n"
    "s_add_u32 s96, s94, 57344\n"
    "s_add_u32 s97, s94, 86016\n"
    "s_lshl_b32 s55, s54, 7\n"
    "v_lshrrev_b32 v2, 3, v255\n"
    "v_lshl_add_u32 v2, v1, 3, v2\n"
    "v_mul_u32_u24 v2, 896, v2\n"
    "v_lshrrev_b32 v3, 4, v255\n"
    "v_and_b32 v4, 1, v1\n"
    "v_lshl_add_u32 v4, v4, 2, v3\n"
    "v_and_b32 v5, 7, v255\n"
    "v_xor_b32 v4, v4, v5\n"
    "v_lshl_add_u32 v230, v4, 4, v2\n"
    "s_waitcnt lgkmcnt(0)\n"
    "v_add_u32 v238, s55, v230\n"
    "s_add_u32 m0, s53, 0\n"
    "s_add_u32 s55, s55, 128\n"
    "buffer_load_dwordx4 v238, s[8:11], s94 offen lds\n"
    "s_add_u32 m0, s53, 4096\n"
    "s_nop 0\n"
    "buffer_load_dwordx4 v238, s[8:11], s95 offen lds\n"
    "s_add_u32 m0, s53, 8192\n"
    "s_cmp_eq_u32 s55, 896\n"
    "s_cselect_b32 s55, 0, s55\n"
    "buffer_load_dwordx4 v238, s[8:11], s96 offen lds\n"
    "s_add_u32 m0, s53, 12288\n"
    "s_nop 0\n"
    "buffer_load_dwordx4 v238, s[8:11], s97 offen lds\n"
    "v_lshl_add_u32 v2, v1, 2, v3\n"
    "v_and_b32 v5, 15, v255\n"
    "v_lshlrev_b32 v4, 2, v3\n"
    "v_xor_b32 v5, v4, v5\n"
    "v_lshlrev_b32 v4, 4, v5\n"
    "v_lshrrev_b32 v5, 2, v5\n"
    "s_xor_b32 s84, s60, 1\n"
    "s_mul_i32 s84, s84, 3\n"
    "v_cmp_ne_u32 s[76:77], s84, v5\n"
    "v_mul_u32_u24 v5, 2688, v2\n"
    "v_add_u32 v231, v5, v4\n"
    "v_mul_u32_u24 v2, 3584, v2\n"
    "v_and_b32 v5, 2, v1\n"
    "v_lshlrev_b32 v5, 4, v5\n"
    "v_xor_b32 v4, v4, v5\n"
    "v_add_u32 v232, v2, v4\n"
    "s_lshl_b32 s84, s50, 7\n"
    "s_mul_i32 s57, s54, 172032\n"
    "s_add_u32 s57, s57, s84\n"
    "s_sub_u32 s57, s57, 43008\n"
    "s_mov_b64 exec, s[76:77]\n"
    "s_add_u32 m0, s53, 16384\n"
    "s_add_u32 s57, s57, 43008\n"
    "buffer_load_dwordx4 v231, s[12:15], s57 offen lds\n"
    "s_add_u32 m0, s53, 20480\n"
    "s_add_u32 s57, s57, 43008\n"
    "buffer_load_dwordx4 v231, s[12:15], s57 offen lds\n"
    "s_add_u32 m0, s53, 24576\n"
    "s_add_u32 s57, s57, 43008\n"
    "buffer_load_dwordx4 v231, s[12:15], s57 offen lds\n"
    "s_add_u32 m0, s53, 28672\n"
    "s_add_u32 s57, s57, 43008\n"
    "buffer_load_dwordx4 v231, s[12:15], s57 offen lds\n"
    "s_mov_b64 exec, -1\n"
    "s_cmp_ge_u32 s57, 1161216\n"
    "s_cselect_b32 s89, 1204224, 0\n"
    "s_sub_u32 s57, s57, s89\n"
    "v_lshlrev_b32 v252, 4, v255\n"
    "s_cmp_eq_u32 s59, 0\n"
    "s_cbranch_scc0 .Lgm_bg_8\n"
    "s_lshl_b32 s85, s48, 8\n"
    "s_mov_b32 m0, 155648\n"
    "s_mov_b64 exec, 0xffff\n"
    "buffer_load_dwordx4 v252, s[20:23], s85 offen lds\n"
    "s_mov_b64 exec, -1\n"
    ".Lgm_bg_8:\n"
    "buffer_load_dwordx2 v[250:251], off, s[24:27], 0 sc1\n"
    "v_add_u32 v238, s55, v230\n"
    "s_add_u32 m0, s53, 49152\n"
    "s_add_u32 s55, s55, 128\n"
    "buffer_load_dwordx4 v238, s[8:11], s94 offen lds\n"
    "s_add_u32 m0, s53, 53248\n"
    "s_nop 0\n"
    "buffer_load_dwordx4 v238, s[8:11], s95 offen lds\n"
    "s_add_u32 m0, s53, 57344\n"
    "s_cmp_eq_u32 s55, 896\n"
    "s_cselect_b32 s55, 0, s55\n"
    "buffer_load_dwordx4 v238, s[8:11], s96 offen lds\n"
    "s_add_u32 m0, s53, 61440\n"
    "s_nop 0\n"
    "buffer_load_dwordx4 v238, s[8:11], s97 offen lds\n"
    "s_mov_b64 exec, s[76:77]\n"
    "s_add_u32 m0, s53, 65536\n"
    "s_add_u32 s57, s57, 43008\n"
    "buffer_load_dwordx4 v231, s[12:15], s57 offen lds\n"
    "s_add_u32 m0, s53, 69632\n"
    "s_add_u32 s57, s57, 43008\n"
    "buffer_load_dwordx4 v231, s[12:15], s57 offen lds\n"
    "s_add_u32 m0, s53, 73728\n"
    "s_add_u32 s57, s57, 43008\n"
    "buffer_load_dwordx4 v231, s[12:15], s57 offen lds\n"
    "s_add_u32 m0, s53, 77824\n"
    "s_add_u32 s57, s57, 43008\n"
    "buffer_load_dwordx4 v231, s[12:15], s57 offen lds\n"
    "s_mov_b64 exec, -1\n"
    "s_cmp_ge_u32 s57, 1161216\n"
    "s_cselect_b32 s89, 1204224, 0\n"
    "s_sub_u32 s57, s57, s89\n"
    "s_lshl_b32 s85, s48, 8\n"
    "s_sub_u32 s58, s85, 57344\n"
    "s_mul_i32 s84, s54, 229376\n"
    "s_add_u32 s58, s58, s84\n"
    "v_lshlrev_b32 v252, 4, v255\n"
    "v_and_b32 v236, 31, v255\n"
    "v_lshrrev_b32 v237, 5, v255\n"
    "v_bfe_u32 v234, v255, 2, 2\n"
    "v_mov_b32 v249, 158720\n"
    "v_mov_b32 v248, 1\n"
    "s_lshl_b32 s63, s47, 2\n"
    "s_add_u32 s63, s63, s59\n"
    "s_lshl_b32 s64, s47, 4\n"
    "s_add_u32 s64, s64, s48\n"
    "s_lshl_b32 s64, s64, 7\n"
    "s_add_u32 s64, s64, 128\n"
    "s_add_u32 s73, s64, 32768\n"
    "s_mul_i32 s84, s46, 37\n"
    "s_lshr_b32 s84, s84, 8\n"
    "s_mul_i32 s85, s84, 7\n"
    "s_sub_u32 s68, s46, s85\n"
    "s_lshl_b32 s67, s45, 2\n"
    "s_add_u32 s67, s67, s84\n"
    "s_mov_b32 s82, 0x3fb8aa3b\n"
    "s_mov_b32 s83, 0x3fb504f3\n"
    "v_lshl_add_u32 v2, v237, 3, v234\n"
    "v_lshlrev_b32 v2, 8, v2\n"
    "v_and_b32 v4, 16, v255\n"
    "v_lshl_add_u32 v2, v4, 1, v2\n"
    "v_and_b32 v4, 1, v255\n"
    "v_lshl_add_u32 v2, v4, 4, v2\n"
    "v_and_b32 v4, 2, v255\n"
    "v_lshl_add_u32 v233, v4, 2, v2\n"
    "s_lshl_b32 s84, s59, 12\n"
    "v_bfe_u32 v2, v255, 1, 3\n"
    "v_xor_b32 v2, v2, v237\n"
    "v_lshlrev_b32 v2, 4, v2\n"
    "v_lshl_add_u32 v2, v236, 7, v2\n"
    "v_add_u32 v224, s84, v2\n"
    "v_xor_b32 v225, 32, v224\n"
    "v_xor_b32 v226, 64, v224\n"
    "v_xor_b32 v227, 0x60, v224\n"
    "v_lshrrev_b32 v3, 4, v255\n"
    "v_and_b32 v5, 15, v255\n"
    "v_lshl_add_u32 v2, v3, 3, v234\n"
    "v_lshlrev_b32 v2, 8, v2\n"
    "v_lshl_add_u32 v2, v234, 6, v2\n"
    "v_and_b32 v4, 16, v255\n"
    "v_lshl_add_u32 v2, v4, 1, v2\n"
    "v_and_b32 v4, 3, v255\n"
    "v_lshl_add_u32 v240, v4, 3, v2\n"
    "v_xor_b32 v241, 32, v240\n"
    "v_xor_b32 v242, 64, v240\n"
    "v_xor_b32 v243, 96, v240\n"
    "v_xor_b32 v244, 128, v240\n"
    "v_xor_b32 v245, 160, v240\n"
    "v_xor_b32 v246, 192, v240\n"
    "v_xor_b32 v247, 224, v240\n"
    "s_lshl_b32 s84, s59, 12\n"
    "v_lshrrev_b32 v2, 1, v5\n"
    "v_xor_b32 v2, v2, v3\n"
    "v_lshlrev_b32 v2, 4, v2\n"
    "v_lshl_add_u32 v2, v5, 7, v2\n"
    "v_add_u32 v224, s84, v2\n"
    "v_xor_b32 v225, 64, v224\n"
    "v_lshlrev_b32 v2, 4, v5\n"
    "v_lshl_add_u32 v239, v3, 9, v2\n"
    "v_lshlrev_b32 v235, 3, v3\n"
    "v_add_u32 v235, 155648, v235\n"
    "s_mul_i32 s50, s63, 114688\n"
    "s_lshl_b32 s84, s48, 13\n"
    "s_add_u32 s50, s50, s84\n"
    "s_add_u32 s50, s50, 5767168\n"
    "s_add_u32 s51, s50, 4096\n"
    "v_lshlrev_b32 v2, 1, v3\n"
    "v_and_b32 v4, 7, v255\n"
    "v_xor_b32 v2, v2, v4\n"
    "v_lshrrev_b32 v4, 3, v255\n"
    "v_mul_u32_u24 v4, 896, v4\n"
    "v_lshl_add_u32 v253, v2, 4, v4\n"
    "s_mul_i32 s52, s59, 100352\n"
    "s_lshl_b32 s85, s68, 7\n"
    "s_add_u32 s52, s52, s85\n"
    "s_mul_i32 s56, s59, 14336\n"
    "s_and_b32 s49, s67, 3\n"
    "s_lshl_b32 s49, s49, 12\n"
    "s_waitcnt vmcnt(9)\n"
    "s_waitcnt lgkmcnt(0)\n"
    "s_barrier\n"
    "ds_read_b128 v[0:3], v224 offset:0\n"
    "ds_read_b128 v[4:7], v224 offset:2048\n"
    "ds_read_b128 v[8:11], v225 offset:0\n"
    "ds_read_b128 v[12:15], v225 offset:2048\n"
    "v_add_u32 v238, s55, v230\n"
    "s_add_u32 m0, s53, 98304\n"
    "s_add_u32 s55, s55, 128\n"
    "buffer_load_dwordx4 v238, s[8:11], s94 offen lds\n"
    "s_add_u32 m0, s53, 102400\n"
    "s_nop 0\n"
    "buffer_load_dwordx4 v238, s[8:11], s95 offen lds\n"
    "s_add_u32 m0, s53, 106496\n"
    "s_cmp_eq_u32 s55, 896\n"
    "s_cselect_b32 s55, 0, s55\n"
    "buffer_load_dwordx4 v238, s[8:11], s96 offen lds\n"
    "s_add_u32 m0, s53, 110592\n"
    "s_nop 0\n"
    "buffer_load_dwordx4 v238, s[8:11], s97 offen lds\n"
    "s_mov_b64 exec, s[76:77]\n"
    "s_add_u32 m0, s53, 114688\n"
    "s_add_u32 s57, s57, 43008\n"
    "buffer_load_dwordx4 v231, s[12:15], s57 offen lds\n"
    "s_add_u32 m0, s53, 118784\n"
    "s_add_u32 s57, s57, 43008\n"
    "buffer_load_dwordx4 v231, s[12:15], s57 offen lds\n"
    "s_add_u32 m0, s53, 122880\n"
    "s_add_u32 s57, s57, 43008\n"
    "buffer_load_dwordx4 v231, s[12:15], s57 offen lds\n"
    "s_add_u32 m0, s53, 126976\n"
    "s_add_u32 s57, s57, 43008\n"
    "buffer_load_dwordx4 v231, s[12:15], s57 offen lds\n"
    "s_mov_b64 exec, -1\n"
    "s_cmp_ge_u32 s57, 1161216\n"
    "s_cselect_b32 s89, 1204224, 0\n"
    "s_sub_u32 s57, s57, s89\n"
    "s_waitcnt vmcnt(8)\n"
    "s_waitcnt lgkmcnt(0)\n"
    "s_barrier\n"
    "ds_read_b128 v[16:19], v224 offset:49152\n"
    "ds_read_b128 v[20:23], v224 offset:51200\n"
    "ds_read_b128 v[24:27], v225 offset:49152\n"
    "ds_read_b128 v[28:31], v225 offset:51200\n"
    "v_add_u32 v238, s55, v230\n"
    "s_add_u32 m0, s53, 0\n"
    "s_add_u32 s55, s55, 128\n"
    "buffer_load_dwordx4 v238, s[8:11], s94 offen lds\n"
    "s_add_u32 m0, s53, 4096\n"
    "s_nop 0\n"
    "buffer_load_dwordx4 v238, s[8:11], s95 offen lds\n"
    "s_add_u32 m0, s53, 8192\n"
    "s_cmp_eq_u32 s55, 896\n"
    "s_cselect_b32 s55, 0, s55\n"
    "buffer_load_dwordx4 v238, s[8:11], s96 offen lds\n"
    "s_add_u32 m0, s53, 12288\n"
    "s_nop 0\n"
    "buffer_load_dwordx4 v238, s[8:11], s97 offen lds\n"
    "s_mov_b64 exec, s[76:77]\n"
    "s_add_u32 m0, s53, 16384\n"
    "s_add_u32 s57, s57, 43008\n"
    "buffer_load_dwordx4 v231, s[12:15], s57 offen lds\n"
    "s_add_u32 m0, s53, 20480\n"
    "s_add_u32 s57, s57, 43008\n"
    "buffer_load_dwordx4 v231, s[12:15], s57 offen lds\n"
    "s_add_u32 m0, s53, 24576\n"
    "s_add_u32 s57, s57, 43008\n"
    "buffer_load_dwordx4 v231, s[12:15], s57 offen lds\n"
    "s_add_u32 m0, s53, 28672\n"
    "s_add_u32 s57, s57, 43008\n"
    "buffer_load_dwordx4 v231, s[12:15], s57 offen lds\n"
    "s_mov_b64 exec, -1\n"
    "s_cmp_ge_u32 s57, 1161216\n"
    "s_cselect_b32 s89, 1204224, 0\n"
    "s_sub_u32 s57, s57, s89\n"
    "v_add_u32 v224, 0x18000, v224\n"
    "v_add_u32 v225, 0x18000, v225\n"
    "s_waitcnt vmcnt(8)\n"
    "s_waitcnt lgkmcnt(0)\n"
    "s_barrier\n"
    "ds_read_b128 v[32:35], v224 offset:0\n"
    "ds_read_b128 v[36:39], v224 offset:2048\n"
    "ds_read_b128 v[40:43], v225 offset:0\n"
    "ds_read_b128 v[44:47], v225 offset:2048\n"
    "v_add_u32 v238, s55, v230\n"
    "s_add_u32 m0, s53, 49152\n"
    "s_add_u32 s55, s55, 128\n"
    "buffer_load_dwordx4 v238, s[8:11], s94 offen lds\n"
    "s_add_u32 m0, s53, 53248\n"
    "s_nop 0\n"
    "buffer_load_dwordx4 v238, s[8:11], s95 offen lds\n"
    "s_add_u32 m0, s53, 57344\n"
    "s_cmp_eq_u32 s55, 896\n"
    "s_cselect_b32 s55, 0, s55\n"
    "buffer_load_dwordx4 v238, s[8:11], s96 offen lds\n"
    "s_add_u32 m0, s53, 61440\n"
    "s_nop 0\n"
    "buffer_load_dwordx4 v238, s[8:11], s97 offen lds\n"
    "s_mov_b64 exec, s[76:77]\n"
    "s_add_u32 m0, s53, 65536\n"
    "s_add_u32 s57, s57, 43008\n"
    "buffer_load_dwordx4 v231, s[12:15], s57 offen lds\n"
    "s_add_u32 m0, s53, 69632\n"
    "s_add_u32 s57, s57, 43008\n"
    "buffer_load_dwordx4 v231, s[12:15], s57 offen lds\n"
    "s_add_u32 m0, s53, 73728\n"
    "s_add_u32 s57, s57, 43008\n"
    "buffer_load_dwordx4 v231, s[12:15], s57 offen lds\n"
    "s_add_u32 m0, s53, 77824\n"
    "s_add_u32 s57, s57, 43008\n"
    "buffer_load_dwordx4 v231, s[12:15], s57 offen lds\n"
    "s_mov_b64 exec, -1\n"
    "s_cmp_ge_u32 s57, 1161216\n"
    "s_cselect_b32 s89, 1204224, 0\n"
    "s_sub_u32 s57, s57, s89\n"
    "v_add_u32 v224, 0xfffe8000, v224\n"
    "v_add_u32 v225, 0xfffe8000, v225\n"
    "s_waitcnt vmcnt(8)\n"
    "s_waitcnt lgkmcnt(0)\n"
    "s_barrier\n"
    "ds_read_b128 v[48:51], v224 offset:0\n"
    "ds_read_b128 v[52:55], v224 offset:2048\n"
    "ds_read_b128 v[56:59], v225 offset:0\n"
    "ds_read_b128 v[60:63], v225 offset:2048\n"
    "s_add_u32 m0, s53, 131072\n"
    "s_add_u32 s58, s58, 57344\n"
    "buffer_load_dwordx4 v232, s[16:19], s58 offen lds\n"
    "ds_read_b64 v[176:177], v235 offset:0\n"
    "s_add_u32 m0, s53, 135168\n"
    "s_add_u32 s58, s58, 57344\n"
    "buffer_load_dwordx4 v232, s[16:19], s58 offen lds\n"
    "ds_read_b64 v[178:179], v235 offset:32\n"
    "s_add_u32 m0, s53, 139264\n"
    "s_add_u32 s58, s58, 57344\n"
    "buffer_load_dwordx4 v232, s[16:19], s58 offen lds\n"
    "ds_read_b64 v[180:181], v235 offset:64\n"
    "s_add_u32 m0, s53, 143360\n"
    "s_add_u32 s58, s58, 57344\n"
    "buffer_load_dwordx4 v232, s[16:19], s58 offen lds\n"
    "s_cmp_ge_u32 s58, 1548288\n"
    "s_cselect_b32 s89, 1605632, 0\n"
    "s_sub_u32 s58, s58, s89\n"
    "ds_read_b64 v[182:183], v235 offset:96\n"
    "v_add_u32 v238, s55, v230\n"
    "s_add_u32 m0, s53, 98304\n"
    "s_add_u32 s55, s55, 128\n"
    "buffer_load_dwordx4 v238, s[8:11], s94 offen lds\n"
    "ds_read_b64 v[184:185], v235 offset:128\n"
    "s_add_u32 m0, s53, 102400\n"
    "s_nop 0\n"
    "buffer_load_dwordx4 v238, s[8:11], s95 offen lds\n"
    "ds_read_b64 v[186:187], v235 offset:160\n"
    "s_add_u32 m0, s53, 106496\n"
    "s_cmp_eq_u32 s55, 896\n"
    "s_cselect_b32 s55, 0, s55\n"
    "buffer_load_dwordx4 v238, s[8:11], s96 offen lds\n"
    "ds_read_b64 v[188:189], v235 offset:192\n"
    "s_add_u32 m0, s53, 110592\n"
    "s_nop 0\n"
    "buffer_load_dwordx4 v238, s[8:11], s97 offen lds\n"
    "ds_read_b64 v[190:191], v235 offset:224\n"
    "s_mov_b64 exec, s[76:77]\n"
    "s_add_u32 m0, s53, 114688\n"
    "s_add_u32 s57, s57, 43008\n"
    "buffer_load_dwordx4 v231, s[12:15], s57 offen lds\n"
    "s_add_u32 m0, s53, 118784\n"
    "s_add_u32 s57, s57, 43008\n"
    "buffer_load_dwordx4 v231, s[12:15], s57 offen lds\n"
    "s_add_u32 m0, s53, 122880\n"
    "s_add_u32 s57, s57, 43008\n"
    "buffer_load_dwordx4 v231, s[12:15], s57 offen lds\n"
    "s_add_u32 m0, s53, 126976\n"
    "s_add_u32 s57, s57, 43008\n"
    "buffer_load_dwordx4 v231, s[12:15], s57 offen lds\n"
    "s_mov_b64 exec, -1\n"
    "s_cmp_ge_u32 s57, 1161216\n"
    "s_cselect_b32 s89, 1204224, 0\n"
    "s_sub_u32 s57, s57, s89\n"
    "v_readfirstlane_b32 s74, v250\n"
    "v_readfirstlane_b32 s75, v251\n"
    "s_add_u32 s74, s74, 0x7f4a7c15\n"
    "s_addc_u32 s75, s75, 0x9e3779b9\n"
    "v_mov_b32 v250, s74\n"
    "v_mov_b32 v251, s75\n"
    "ds_write_b64 v249, v[250:251] offset:64\n"
    "s_waitcnt vmcnt(12)\n"
    "s_waitcnt lgkmcnt(0)\n"
    "s_barrier\n"
    "ds_read_b128 v[64:67], v224 offset:49152\n"
    "ds_read_b128 v[68:71], v224 offset:51200\n"
    "ds_read_b128 v[72:75], v225 offset:49152\n"
    "ds_read_b128 v[76:79], v225 offset:51200\n"
    "s_add_u32 m0, s53, 32768\n"
    "s_add_u32 s58, s58, 57344\n"
    "buffer_load_dwordx4 v232, s[16:19], s58 offen lds\n"
    "v_lshlrev_b32 v112, 16, v176\n"
    "v_and_b32 v113, 0xffff0000, v176\n"
    "v_lshlrev_b32 v114, 16, v177\n"
    "v_and_b32 v115, 0xffff0000, v177\n"
    "v_lshlrev_b32 v120, 16, v176\n"
    "v_and_b32 v121, 0xffff0000, v176\n"
    "v_lshlrev_b32 v122, 16, v177\n"
    "v_and_b32 v123, 0xffff0000, v177\n"
    "s_add_u32 m0, s53, 36864\n"
    "s_add_u32 s58, s58, 57344\n"
    "buffer_load_dwordx4 v232, s[16:19], s58 offen lds\n"
    "v_lshlrev_b32 v116, 16, v178\n"
    "v_and_b32 v117, 0xffff0000, v178\n"
    "v_lshlrev_b32 v118, 16, v179\n"
    "v_and_b32 v119, 0xffff0000, v179\n"
    "v_lshlrev_b32 v124, 16, v178\n"
    "v_and_b32 v125, 0xffff0000, v178\n"
    "v_lshlrev_b32 v126, 16, v179\n"
    "v_and_b32 v127, 0xffff0000, v179\n"
    "s_add_u32 m0, s53, 40960\n"
    "s_add_u32 s58, s58, 57344\n"
    "buffer_load_dwordx4 v232, s[16:19], s58 offen lds\n"
    "v_lshlrev_b32 v128, 16, v180\n"
    "v_and_b32 v129, 0xffff0000, v180\n"
    "v_lshlrev_b32 v130, 16, v181\n"
    "v_and_b32 v131, 0xffff0000, v181\n"
    "v_lshlrev_b32 v136, 16, v180\n"
    "v_and_b32 v137, 0xffff0000, v180\n"
    "v_lshlrev_b32 v138, 16, v181\n"
    "v_and_b32 v139, 0xffff0000, v181\n"
    "s_add_u32 m0, s53, 45056\n"
    "s_add_u32 s58, s58, 57344\n"
    "buffer_load_dwordx4 v232, s[16:19], s58 offen lds\n"
    "s_cmp_ge_u32 s58, 1548288\n"
    "s_cselect_b32 s89, 1605632, 0\n"
    "s_sub_u32 s58, s58, s89\n"
    "v_lshlrev_b32 v132, 16, v182\n"
    "v_and_b32 v133, 0xffff0000, v182\n"
    "v_lshlrev_b32 v134, 16, v183\n"
    "v_and_b32 v135, 0xffff0000, v183\n"
    "v_lshlrev_b32 v140, 16, v182\n"
    "v_and_b32 v141, 0xffff0000, v182\n"
    "v_lshlrev_b32 v142, 16, v183\n"
    "v_and_b32 v143, 0xffff0000, v183\n"
    "v_add_u32 v238, s55, v230\n"
    "s_add_u32 m0, s53, 0\n"
    "s_add_u32 s55, s55, 128\n"
    "buffer_load_dwordx4 v238, s[8:11], s94 offen lds\n"
    "v_lshlrev_b32 v144, 16, v184\n"
    "v_and_b32 v145, 0xffff0000, v184\n"
    "v_lshlrev_b32 v146, 16, v185\n"
    "v_and_b32 v147, 0xffff0000, v185\n"
    "v_lshlrev_b32 v152, 16, v184\n"
    "v_and_b32 v153, 0xffff0000, v184\n"
    "v_lshlrev_b32 v154, 16, v185\n"
    "v_and_b32 v155, 0xffff0000, v185\n"
    "s_add_u32 m0, s53, 4096\n"
    "s_nop 0\n"
    "buffer_load_dwordx4 v238, s[8:11], s95 offen lds\n"
    "v_lshlrev_b32 v148, 16, v186\n"
    "v_and_b32 v149, 0xffff0000, v186\n"
    "v_lshlrev_b32 v150, 16, v187\n"
    "v_and_b32 v151, 0xffff0000, v187\n"
    "v_lshlrev_b32 v156, 16, v186\n"
    "v_and_b32 v157, 0xffff0000, v186\n"
    "v_lshlrev_b32 v158, 16, v187\n"
    "v_and_b32 v159, 0xffff0000, v187\n"
    "s_add_u32 m0, s53, 8192\n"
    "s_cmp_eq_u32 s55, 896\n"
    "s_cselect_b32 s55, 0, s55\n"
    "buffer_load_dwordx4 v238, s[8:11], s96 offen lds\n"
    "v_lshlrev_b32 v160, 16, v188\n"
    "v_and_b32 v161, 0xffff0000, v188\n"
    "v_lshlrev_b32 v162, 16, v189\n"
    "v_and_b32 v163, 0xffff0000, v189\n"
    "v_lshlrev_b32 v168, 16, v188\n"
    "v_and_b32 v169, 0xffff0000, v188\n"
    "v_lshlrev_b32 v170, 16, v189\n"
    "v_and_b32 v171, 0xffff0000, v189\n"
    "s_add_u32 m0, s53, 12288\n"
    "s_nop 0\n"
    "buffer_load_dwordx4 v238, s[8:11], s97 offen lds\n"
    "v_lshlrev_b32 v164, 16, v190\n"
    "v_and_b32 v165, 0xffff0000, v190\n"
    "v_lshlrev_b32 v166, 16, v191\n"
    "v_and_b32 v167, 0xffff0000, v191\n"
    "v_lshlrev_b32 v172, 16, v190\n"
    "v_and_b32 v173, 0xffff0000, v190\n"
    "v_lshlrev_b32 v174, 16, v191\n"
    "v_and_b32 v175, 0xffff0000, v191\n"
    "s_mov_b64 exec, s[76:77]\n"
    "s_add_u32 m0, s53, 16384\n"
    "s_add_u32 s57, s57, 43008\n"
    "buffer_load_dwordx4 v231, s[12:15], s57 offen lds\n"
    "s_add_u32 m0, s53, 20480\n"
    "s_add_u32 s57, s57, 43008\n"
    "buffer_load_dwordx4 v231, s[12:15], s57 offen lds\n"
    "s_add_u32 m0, s53, 24576\n"
    "s_add_u32 s57, s57, 43008\n"
    "buffer_load_dwordx4 v231, s[12:15], s57 offen lds\n"
    "s_add_u32 m0, s53, 28672\n"
    "s_add_u32 s57, s57, 43008\n"
    "buffer_load_dwordx4 v231, s[12:15], s57 offen lds\n"
    "s_mov_b64 exec, -1\n"
    "v_add_u32 v224, 0x18000, v224\n"
    "v_add_u32 v225, 0x18000, v225\n"
    "s_waitcnt vmcnt(12)\n"
    "s_waitcnt lgkmcnt(0)\n"
    "s_barrier\n"
    "s_add_u32 m0, s53, 49152\n"
    "s_add_u32 s58, s58, 57344\n"
    "buffer_load_dwordx4 v232, s[16:19], s58 offen lds\n"
    "v_add_u32 v240, 0x20000, v240\n"
    "ds_read_b64_tr_b16 v[176:177], v240 offset:0\n"
    "ds_read_b64_tr_b16 v[178:179], v240 offset:1024\n"
    "s_add_u32 m0, s53, 53248\n"
    "s_add_u32 s58, s58, 57344\n"
    "buffer_load_dwordx4 v232, s[16:19], s58 offen lds\n"
    "v_add_u32 v241, 0x20000, v241\n"
    "ds_read_b64_tr_b16 v[180:181], v241 offset:0\n"
    "ds_read_b64_tr_b16 v[182:183], v241 offset:1024\n"
    "s_add_u32 m0, s53, 57344\n"
    "s_add_u32 s58, s58, 57344\n"
    "buffer_load_dwordx4 v232, s[16:19], s58 offen lds\n"
    "v_add_u32 v242, 0x20000, v242\n"
    "ds_read_b64_tr_b16 v[184:185], v242 offset:0\n"
    "ds_read_b64_tr_b16 v[186:187], v242 offset:1024\n"
    "ds_read_b128 v[80:83], v224 offset:0\n"
    "v_add_u32 v243, 0x20000, v243\n"
    "ds_read_b64_tr_b16 v[188:189], v243 offset:0\n"
    "ds_read_b64_tr_b16 v[190:191], v243 offset:1024\n"
    "s_add_u32 m0, s53, 61440\n"
    "s_add_u32 s58, s58, 57344\n"
    "buffer_load_dwordx4 v232, s[16:19], s58 offen lds\n"
    "s_cmp_ge_u32 s58, 1548288\n"
    "s_cselect_b32 s89, 1605632, 0\n"
    "s_sub_u32 s58, s58, s89\n"
    "v_add_u32 v244, 0x20000, v244\n"
    "ds_read_b64_tr_b16 v[192:193], v244 offset:0\n"
    "ds_read_b64_tr_b16 v[194:195], v244 offset:1024\n"
    "ds_read_b128 v[84:87], v224 offset:2048\n"
    "s_add_u32 m0, s53, 65536\n"
    "s_add_u32 s58, s58, 57344\n"
    "buffer_load_dwordx4 v232, s[16:19], s58 offen lds\n"
    "v_add_u32 v245, 0x20000, v245\n"
    "ds_read_b64_tr_b16 v[196:197], v245 offset:0\n"
    "ds_read_b64_tr_b16 v[198:199], v245 offset:1024\n"
    "s_add_u32 m0, s53, 69632\n"
    "s_add_u32 s58, s58, 57344\n"
    "buffer_load_dwordx4 v232, s[16:19], s58 offen lds\n"
    "s_waitcnt lgkmcnt(10)\n"
    "v_add_u32 v246, 0x20000, v246\n"
    "v_mfma_f32_16x16x32_bf16 v[112:115], v[176:179], v[0:3], v[112:115]\n"
    "ds_read_b64_tr_b16 v[200:201], v246 offset:0\n"
    "v_mfma_f32_16x16x32_bf16 v[120:123], v[176:179], v[4:7], v[120:123]\n"
    "ds_read_b64_tr_b16 v[202:203], v246 offset:1024\n"
    "ds_read_b128 v[88:91], v225 offset:0\n"
    "v_add_u32 v247, 0x20000, v247\n"
    "v_mfma_f32_16x16x32_bf16 v[116:119], v[180:183], v[0:3], v[116:119]\n"
    "ds_read_b64_tr_b16 v[204:205], v247 offset:0\n"
    "v_mfma_f32_16x16x32_bf16 v[124:127], v[180:183], v[4:7], v[124:127]\n"
    "ds_read_b64_tr_b16 v[206:207], v247 offset:1024\n"
    "s_add_u32 m0, s53, 73728\n"
    "s_add_u32 s58, s58, 57344\n"
    "buffer_load_dwordx4 v232, s[16:19], s58 offen lds\n"
    "s_waitcnt lgkmcnt(10)\n"
    "v_mfma_f32_16x16x32_bf16 v[128:131], v[184:187], v[0:3], v[128:131]\n"
    "ds_read_b64_tr_b16 v[176:177], v240 offset:8192\n"
    "v_mfma_f32_16x16x32_bf16 v[136:139], v[184:187], v[4:7], v[136:139]\n"
    "ds_read_b64_tr_b16 v[178:179], v240 offset:9216\n"
    "ds_read_b128 v[92:95], v225 offset:2048\n"
    "s_add_u32 m0, s53, 77824\n"
    "s_add_u32 s58, s58, 57344\n"
    "buffer_load_dwordx4 v232, s[16:19], s58 offen lds\n"
    "s_cmp_ge_u32 s58, 1548288\n"
    "s_cselect_b32 s89, 1605632, 0\n"
    "s_sub_u32 s58, s58, s89\n"
    "v_mfma_f32_16x16x32_bf16 v[132:135], v[188:191], v[0:3], v[132:135]\n"
    "ds_read_b64_tr_b16 v[180:181], v241 offset:8192\n"
    "v_mfma_f32_16x16x32_bf16 v[140:143], v[188:191], v[4:7], v[140:143]\n"
    "ds_read_b64_tr_b16 v[182:183], v241 offset:9216\n"
    "s_add_u32 m0, s53, 81920\n"
    "s_add_u32 s58, s58, 57344\n"
    "buffer_load_dwordx4 v232, s[16:19], s58 offen lds\n"
    "s_waitcnt lgkmcnt(10)\n"
    "v_mfma_f32_16x16x32_bf16 v[144:147], v[192:195], v[0:3], v[144:147]\n"
    "ds_read_b64_tr_b16 v[184:185], v242 offset:8192\n"
    "v_mfma_f32_16x16x32_bf16 v[152:155], v[192:195], v[4:7], v[152:155]\n"
    "ds_read_b64_tr_b16 v[186:187], v242 offset:9216\n"
    "v_mfma_f32_16x16x32_bf16 v[148:151], v[196:199], v[0:3], v[148:151]\n"
    "ds_read_b64_tr_b16 v[188:189], v243 offset:8192\n"
    "v_mfma_f32_16x16x32_bf16 v[156:159], v[196:199], v[4:7], v[156:159]\n"
    "ds_read_b64_tr_b16 v[190:191], v243 offset:9216\n"
    "s_add_u32 m0, s53, 86016\n"
    "s_add_u32 s58, s58, 57344\n"
    "buffer_load_dwordx4 v232, s[16:19], s58 offen lds\n"
    "s_waitcnt lgkmcnt(9)\n"
    "v_mfma_f32_16x16x32_bf16 v[160:163], v[200:203], v[0:3], v[160:163]\n"
    "ds_read_b64_tr_b16 v[192:193], v244 offset:8192\n"
    "v_mfma_f32_16x16x32_bf16 v[168:171], v[200:203], v[4:7], v[168:171]\n"
    "ds_read_b64_tr_b16 v[194:195], v244 offset:9216\n"
    "s_add_u32 m0, s53, 90112\n"
    "s_add_u32 s58, s58, 57344\n"
    "buffer_load_dwordx4 v232, s[16:19], s58 offen lds\n"
    "v_mfma_f32_16x16x32_bf16 v[164:167], v[204:207], v[0:3], v[164:167]\n"
    "ds_read_b64_tr_b16 v[196:197], v245 offset:8192\n"
    "v_mfma_f32_16x16x32_bf16 v[172:175], v[204:207], v[4:7], v[172:175]\n"
    "ds_read_b64_tr_b16 v[198:199], v245 offset:9216\n"
    "s_add_u32 m0, s53, 94208\n"
    "s_add_u32 s58, s58, 57344\n"
    "buffer_load_dwordx4 v232, s[16:19], s58 offen lds\n"
    "s_cmp_ge_u32 s58, 1548288\n"
    "s_cselect_b32 s89, 1605632, 0\n"
    "s_sub_u32 s58, s58, s89\n"
    "s_waitcnt lgkmcnt(8)\n"
    "v_mfma_f32_16x16x32_bf16 v[112:115], v[176:179], v[8:11], v[112:115]\n"
    "ds_read_b64_tr_b16 v[200:201], v246 offset:8192\n"
    "v_mfma_f32_16x16x32_bf16 v[120:123], v[176:179], v[12:15], v[120:123]\n"
    "ds_read_b64_tr_b16 v[202:203], v246 offset:9216\n"
    "v_mfma_f32_16x16x32_bf16 v[116:119], v[180:183], v[8:11], v[116:119]\n"
    "ds_read_b64_tr_b16 v[204:205], v247 offset:8192\n"
    "v_mfma_f32_16x16x32_bf16 v[124:127], v[180:183], v[12:15], v[124:127]\n"
    "ds_read_b64_tr_b16 v[206:207], v247 offset:9216\n"
    "v_add_u32 v224, 0xfffe8000, v224\n"
    "v_add_u32 v225, 0xfffe8000, v225\n"
    "s_waitcnt vmcnt(12)\n"
    "s_waitcnt lgkmcnt(0)\n"
    "s_barrier\n"
    "s_setprio 0\n"
    "s_add_u32 m0, s53, 98304\n"
    "s_add_u32 s58, s58, 57344\n"
    "buffer_load_dwordx4 v232, s[16:19], s58 offen lds\n"
    "ds_read_b128 v[96:99], v224 offset:0\n"
    "s_add_u32 m0, s53, 102400\n"
    "s_add_u32 s58, s58, 57344\n"
    "buffer_load_dwordx4 v232, s[16:19], s58 offen lds\n"
    "ds_read_b128 v[100:103], v224 offset:2048\n"
    "s_add_u32 m0, s53, 106496\n"
    "s_add_u32 s58, s58, 57344\n"
    "buffer_load_dwordx4 v232, s[16:19], s58 offen lds\n"
    "ds_read_b128 v[104:107], v225 offset:0\n"
    "s_add_u32 m0, s53, 110592\n"
    "s_add_u32 s58, s58, 57344\n"
    "buffer_load_dwordx4 v232, s[16:19], s58 offen lds\n"
    "s_cmp_ge_u32 s58, 1548288\n"
    "s_cselect_b32 s89, 1605632, 0\n"
    "s_sub_u32 s58, s58, s89\n"
    "ds_read_b128 v[108:111], v225 offset:2048\n"
    "s_add_u32 m0, s53, 114688\n"
    "s_add_u32 s58, s58, 57344\n"
    "buffer_load_dwordx4 v232, s[16:19], s58 offen lds\n"
    "s_add_u32 m0, s53, 118784\n"
    "s_add_u32 s58, s58, 57344\n"
    "buffer_load_dwordx4 v232, s[16:19], s58 offen lds\n"
    "s_add_u32 m0, s53, 122880\n"
    "s_add_u32 s58, s58, 57344\n"
    "buffer_load_dwordx4 v232, s[16:19], s58 offen lds\n"
    "s_add_u32 m0, s53, 126976\n"
    "s_add_u32 s58, s58, 57344\n"
    "buffer_load_dwordx4 v232, s[16:19], s58 offen lds\n"
    "s_waitcnt vmcnt(8)\n"
    "s_mov_b64 exec, 1\n"
    "ds_add_u32 v249, v248 offset:24\n"
    "s_mov_b64 exec, -1\n"
    "v_add_u32 v240, 0xfffe8000, v240\n"
    "v_mfma_f32_16x16x32_bf16 v[128:131], v[184:187], v[8:11], v[128:131]\n"
    "ds_read_b64_tr_b16 v[176:177], v240 offset:0\n"
    "v_mfma_f32_16x16x32_bf16 v[136:139], v[184:187], v[12:15], v[136:139]\n"
    "ds_read_b64_tr_b16 v[178:179], v240 offset:1024\n"
    "v_add_u32 v241, 0xfffe8000, v241\n"
    "v_mfma_f32_16x16x32_bf16 v[132:135], v[188:191], v[8:11], v[132:135]\n"
    "ds_read_b64_tr_b16 v[180:181], v241 offset:0\n"
    "v_mfma_f32_16x16x32_bf16 v[140:143], v[188:191], v[12:15], v[140:143]\n"
    "ds_read_b64_tr_b16 v[182:183], v241 offset:1024\n"
    "v_add_u32 v242, 0xfffe8000, v242\n"
    "v_mfma_f32_16x16x32_bf16 v[144:147], v[192:195], v[8:11], v[144:147]\n"
    "ds_read_b64_tr_b16 v[184:185], v242 offset:0\n"
    "v_mfma_f32_16x16x32_bf16 v[152:155], v[192:195], v[12:15], v[152:155]\n"
    "ds_read_b64_tr_b16 v[186:187], v242 offset:1024\n"
    "v_add_u32 v243, 0xfffe8000, v243\n"
    "v_mfma_f32_16x16x32_bf16 v[148:151], v[196:199], v[8:11], v[148:151]\n"
    "ds_read_b64_tr_b16 v[188:189], v243 offset:0\n"
    "v_mfma_f32_16x16x32_bf16 v[156:159], v[196:199], v[12:15], v[156:159]\n"
    "ds_read_b64_tr_b16 v[190:191], v243 offset:1024\n"
    "v_add_u32 v244, 0xfffe8000, v244\n"
    "v_mfma_f32_16x16x32_bf16 v[160:163], v[200:203], v[8:11], v[160:163]\n"
    "ds_read_b64_tr_b16 v[192:193], v244 offset:0\n"
    "v_mfma_f32_16x16x32_bf16 v[168:171], v[200:203], v[12:15], v[168:171]\n"
    "ds_read_b64_tr_b16 v[194:195], v244 offset:1024\n"
    "v_add_u32 v245, 0xfffe8000, v245\n"
    "v_mfma_f32_16x16x32_bf16 v[164:167], v[204:207], v[8:11], v[164:167]\n"
    "ds_read_b64_tr_b16 v[196:197], v245 offset:0\n"
    "v_mfma_f32_16x16x32_bf16 v[172:175], v[204:207], v[12:15], v[172:175]\n"
    "ds_read_b64_tr_b16 v[198:199], v245 offset:1024\n"
    "s_waitcnt lgkmcnt(8)\n"
    "v_add_u32 v246, 0xfffe8000, v246\n"
    "v_mfma_f32_16x16x32_bf16 v[112:115], v[176:179], v[16:19], v[112:115]\n"
    "ds_read_b64_tr_b16 v[200:201], v246 offset:0\n"
    "v_mfma_f32_16x16x32_bf16 v[120:123], v[176:179], v[20:23], v[120:123]\n"
    "ds_read_b64_tr_b16 v[202:203], v246 offset:1024\n"
    "v_add_u32 v247, 0xfffe8000, v247\n"
    "v_mfma_f32_16x16x32_bf16 v[116:119], v[180:183], v[16:19], v[116:119]\n"
    "ds_read_b64_tr_b16 v[204:205], v247 offset:0\n"
    "v_mfma_f32_16x16x32_bf16 v[124:127], v[180:183], v[20:23], v[124:127]\n"
    "ds_read_b64_tr_b16 v[206:207], v247 offset:1024\n"
    "s_waitcnt lgkmcnt(8)\n"
    "v_mfma_f32_16x16x32_bf16 v[128:131], v[184:187], v[16:19], v[128:131]\n"
    "ds_read_b64_tr_b16 v[176:177], v240 offset:8192\n"
    "v_mfma_f32_16x16x32_bf16 v[136:139], v[184:187], v[20:23], v[136:139]\n"
    "ds_read_b64_tr_b16 v[178:179], v240 offset:9216\n"
    "v_mfma_f32_16x16x32_bf16 v[132:135], v[188:191], v[16:19], v[132:135]\n"
    "ds_read_b64_tr_b16 v[180:181], v241 offset:8192\n"
    "v_mfma_f32_16x16x32_bf16 v[140:143], v[188:191], v[20:23], v[140:143]\n"
    "ds_read_b64_tr_b16 v[182:183], v241 offset:9216\n"
    "s_waitcnt lgkmcnt(8)\n"
    "v_mfma_f32_16x16x32_bf16 v[144:147], v[192:195], v[16:19], v[144:147]\n"
    "ds_read_b64_tr_b16 v[184:185], v242 offset:8192\n"
    "v_mfma_f32_16x16x32_bf16 v[152:155], v[192:195], v[20:23], v[152:155]\n"
    "ds_read_b64_tr_b16 v[186:187], v242 offset:9216\n"
    "v_mfma_f32_16x16x32_bf16 v[148:151], v[196:199], v[16:19], v[148:151]\n"
    "ds_read_b64_tr_b16 v[188:189], v243 offset:8192\n"
    "v_mfma_f32_16x16x32_bf16 v[156:159], v[196:199], v[20:23], v[156:159]\n"
    "ds_read_b64_tr_b16 v[190:191], v243 offset:9216\n"
    "s_waitcnt lgkmcnt(8)\n"
    "v_mfma_f32_16x16x32_bf16 v[160:163], v[200:203], v[16:19], v[160:163]\n"
    "ds_read_b64_tr_b16 v[192:193], v244 offset:8192\n"
    "v_mfma_f32_16x16x32_bf16 v[168:171], v[200:203], v[20:23], v[168:171]\n"
    "ds_read_b64_tr_b16 v[194:195], v244 offset:9216\n"
    "v_mfma_f32_16x16x32_bf16 v[164:167], v[204:207], v[16:19], v[164:167]\n"
    "ds_read_b64_tr_b16 v[196:197], v245 offset:8192\n"
    "v_mfma_f32_16x16x32_bf16 v[172:175], v[204:207], v[20:23], v[172:175]\n"
    "ds_read_b64_tr_b16 v[198:199], v245 offset:9216\n"
    "s_waitcnt lgkmcnt(8)\n"
    "v_mfma_f32_16x16x32_bf16 v[112:115], v[176:179], v[24:27], v[112:115]\n"
    "ds_read_b64_tr_b16 v[200:201], v246 offset:8192\n"
    "v_mfma_f32_16x16x32_bf16 v[120:123], v[176:179], v[28:31], v[120:123]\n"
    "ds_read_b64_tr_b16 v[202:203], v246 offset:9216\n"
    "v_mfma_f32_16x16x32_bf16 v[116:119], v[180:183], v[24:27], v[116:119]\n"
    "ds_read_b64_tr_b16 v[204:205], v247 offset:8192\n"
    "v_mfma_f32_16x16x32_bf16 v[124:127], v[180:183], v[28:31], v[124:127]\n"
    "ds_read_b64_tr_b16 v[206:207], v247 offset:9216\n"
    "s_setprio 1\n"
    "ds_read_b32 v228, v249 offset:24\n"
    "s_waitcnt lgkmcnt(9)\n"
    "v_mfma_f32_16x16x32_bf16 v[128:131], v[184:187], v[24:27], v[128:131]\n"
    "v_mfma_f32_16x16x32_bf16 v[136:139], v[184:187], v[28:31], v[136:139]\n"
    "v_mfma_f32_16x16x32_bf16 v[132:135], v[188:191], v[24:27], v[132:135]\n"
    "v_mfma_f32_16x16x32_bf16 v[140:143], v[188:191], v[28:31], v[140:143]\n"
    "s_waitcnt lgkmcnt(7)\n"
    "v_mfma_f32_16x16x32_bf16 v[144:147], v[192:195], v[24:27], v[144:147]\n"
    "v_mfma_f32_16x16x32_bf16 v[152:155], v[192:195], v[28:31], v[152:155]\n"
    "s_waitcnt lgkmcnt(0)\n"
    "v_readfirstlane_b32 s91, v228\n"
    "s_cmp_ge_u32 s91, 4\n"
    "s_cbranch_scc0 .Lgm_lc_10\n"
    ".Lgm_lcb_11:\n"
    "ds_read_b64_tr_b16 v[176:177], v240 offset:16384\n"
    "ds_read_b64_tr_b16 v[178:179], v240 offset:17408\n"
    "ds_read_b64_tr_b16 v[180:181], v241 offset:16384\n"
    "ds_read_b64_tr_b16 v[182:183], v241 offset:17408\n"
    "ds_read_b64_tr_b16 v[184:185], v242 offset:16384\n"
    "ds_read_b64_tr_b16 v[186:187], v242 offset:17408\n"
    "v_mfma_f32_16x16x32_bf16 v[148:151], v[196:199], v[24:27], v[148:151]\n"
    "ds_read_b64_tr_b16 v[188:189], v243 offset:16384\n"
    "v_mfma_f32_16x16x32_bf16 v[156:159], v[196:199], v[28:31], v[156:159]\n"
    "ds_read_b64_tr_b16 v[190:191], v243 offset:17408\n"
    "v_mfma_f32_16x16x32_bf16 v[160:163], v[200:203], v[24:27], v[160:163]\n"
    "ds_read_b64_tr_b16 v[192:193], v244 offset:16384\n"
    "v_mfma_f32_16x16x32_bf16 v[168:171], v[200:203], v[28:31], v[168:171]\n"
    "ds_read_b64_tr_b16 v[194:195], v244 offset:17408\n"
    "v_mfma_f32_16x16x32_bf16 v[164:167], v[204:207], v[24:27], v[164:167]\n"
    "ds_read_b64_tr_b16 v[196:197], v245 offset:16384\n"
    "v_mfma_f32_16x16x32_bf16 v[172:175], v[204:207], v[28:31], v[172:175]\n"
    "ds_read_b64_tr_b16 v[198:199], v245 offset:17408\n"
    "s_waitcnt lgkmcnt(8)\n"
    "v_mfma_f32_16x16x32_bf16 v[112:115], v[176:179], v[32:35], v[112:115]\n"
    "ds_read_b64_tr_b16 v[200:201], v246 offset:16384\n"
    "v_mfma_f32_16x16x32_bf16 v[120:123], v[176:179], v[36:39], v[120:123]\n"
    "ds_read_b64_tr_b16 v[202:203], v246 offset:17408\n"
    "v_mfma_f32_16x16x32_bf16 v[116:119], v[180:183], v[32:35], v[116:119]\n"
    "ds_read_b64_tr_b16 v[204:205], v247 offset:16384\n"
    "v_mfma_f32_16x16x32_bf16 v[124:127], v[180:183], v[36:39], v[124:127]\n"
    "ds_read_b64_tr_b16 v[206:207], v247 offset:17408\n"
    "s_waitcnt lgkmcnt(8)\n"
    "v_mfma_f32_16x16x32_bf16 v[128:131], v[184:187], v[32:35], v[128:131]\n"
    "ds_read_b64_tr_b16 v[176:177], v240 offset:24576\n"
    "v_mfma_f32_16x16x32_bf16 v[136:139], v[184:187], v[36:39], v[136:139]\n"
    "ds_read_b64_tr_b16 v[178:179], v240 offset:25600\n"
    "v_mfma_f32_16x16x32_bf16 v[132:135], v[188:191], v[32:35], v[132:135]\n"
    "ds_read_b64_tr_b16 v[180:181], v241 offset:24576\n"
    "v_mfma_f32_16x16x32_bf16 v[140:143], v[188:191], v[36:39], v[140:143]\n"
    "ds_read_b64_tr_b16 v[182:183], v241 offset:25600\n"
    "s_waitcnt lgkmcnt(8)\n"
    "v_mfma_f32_16x16x32_bf16 v[144:147], v[192:195], v[32:35], v[144:147]\n"
    "ds_read_b64_tr_b16 v[184:185], v242 offset:24576\n"
    "v_mfma_f32_16x16x32_bf16 v[152:155], v[192:195], v[36:39], v[152:155]\n"
    "ds_read_b64_tr_b16 v[186:187], v242 offset:25600\n"
    "v_mfma_f32_16x16x32_bf16 v[148:151], v[196:199], v[32:35], v[148:151]\n"
    "ds_read_b64_tr_b16 v[188:189], v243 offset:24576\n"
    "v_mfma_f32_16x16x32_bf16 v[156:159], v[196:199], v[36:39], v[156:159]\n"
    "ds_read_b64_tr_b16 v[190:191], v243 offset:25600\n"
    "s_waitcnt lgkmcnt(8)\n"
    "v_mfma_f32_16x16x32_bf16 v[160:163], v[200:203], v[32:35], v[160:163]\n"
    "ds_read_b64_tr_b16 v[192:193], v244 offset:24576\n"
    "v_mfma_f32_16x16x32_bf16 v[168:171], v[200:203], v[36:39], v[168:171]\n"
    "ds_read_b64_tr_b16 v[194:195], v244 offset:25600\n"
    "v_mfma_f32_16x16x32_bf16 v[164:167], v[204:207], v[32:35], v[164:167]\n"
    "ds_read_b64_tr_b16 v[196:197], v245 offset:24576\n"
    "v_mfma_f32_16x16x32_bf16 v[172:175], v[204:207], v[36:39], v[172:175]\n"
    "ds_read_b64_tr_b16 v[198:199], v245 offset:25600\n"
    "s_waitcnt lgkmcnt(8)\n"
    "v_mfma_f32_16x16x32_bf16 v[112:115], v[176:179], v[40:43], v[112:115]\n"
    "ds_read_b64_tr_b16 v[200:201], v246 offset:24576\n"
    "v_mfma_f32_16x16x32_bf16 v[120:123], v[176:179], v[44:47], v[120:123]\n"
    "ds_read_b64_tr_b16 v[202:203], v246 offset:25600\n"
    "v_mfma_f32_16x16x32_bf16 v[116:119], v[180:183], v[40:43], v[116:119]\n"
    "ds_read_b64_tr_b16 v[204:205], v247 offset:24576\n"
    "v_mfma_f32_16x16x32_bf16 v[124:127], v[180:183], v[44:47], v[124:127]\n"
    "ds_read_b64_tr_b16 v[206:207], v247 offset:25600\n"
    "s_waitcnt lgkmcnt(8)\n"
    "v_mfma_f32_16x16x32_bf16 v[128:131], v[184:187], v[40:43], v[128:131]\n"
    "ds_read_b64_tr_b16 v[176:177], v240 offset:32768\n"
    "v_mfma_f32_16x16x32_bf16 v[136:139], v[184:187], v[44:47], v[136:139]\n"
    "ds_read_b64_tr_b16 v[178:179], v240 offset:33792\n"
    "v_mfma_f32_16x16x32_bf16 v[132:135], v[188:191], v[40:43], v[132:135]\n"
    "ds_read_b64_tr_b16 v[180:181], v241 offset:32768\n"
    "v_mfma_f32_16x16x32_bf16 v[140:143], v[188:191], v[44:47], v[140:143]\n"
    "ds_read_b64_tr_b16 v[182:183], v241 offset:33792\n"
    "s_waitcnt lgkmcnt(8)\n"
    "v_mfma_f32_16x16x32_bf16 v[144:147], v[192:195], v[40:43], v[144:147]\n"
    "ds_read_b64_tr_b16 v[184:185], v242 offset:32768\n"
    "v_mfma_f32_16x16x32_bf16 v[152:155], v[192:195], v[44:47], v[152:155]\n"
    "ds_read_b64_tr_b16 v[186:187], v242 offset:33792\n"
    "v_mfma_f32_16x16x32_bf16 v[148:151], v[196:199], v[40:43], v[148:151]\n"
    "ds_read_b64_tr_b16 v[188:189], v243 offset:32768\n"
    "v_mfma_f32_16x16x32_bf16 v[156:159], v[196:199], v[44:47], v[156:159]\n"
    "ds_read_b64_tr_b16 v[190:191], v243 offset:33792\n"
    "s_waitcnt lgkmcnt(8)\n"
    "v_mfma_f32_16x16x32_bf16 v[160:163], v[200:203], v[40:43], v[160:163]\n"
    "ds_read_b64_tr_b16 v[192:193], v244 offset:32768\n"
    "v_mfma_f32_16x16x32_bf16 v[168:171], v[200:203], v[44:47], v[168:171]\n"
    "ds_read_b64_tr_b16 v[194:195], v244 offset:33792\n"
    "v_mfma_f32_16x16x32_bf16 v[164:167], v[204:207], v[40:43], v[164:167]\n"
    "ds_read_b64_tr_b16 v[196:197], v245 offset:32768\n"
    "v_mfma_f32_16x16x32_bf16 v[172:175], v[204:207], v[44:47], v[172:175]\n"
    "ds_read_b64_tr_b16 v[198:199], v245 offset:33792\n"
    "s_waitcnt lgkmcnt(8)\n"
    "v_mfma_f32_16x16x32_bf16 v[112:115], v[176:179], v[48:51], v[112:115]\n"
    "ds_read_b64_tr_b16 v[200:201], v246 offset:32768\n"
    "v_mfma_f32_16x16x32_bf16 v[120:123], v[176:179], v[52:55], v[120:123]\n"
    "ds_read_b64_tr_b16 v[202:203], v246 offset:33792\n"
    "v_mfma_f32_16x16x32_bf16 v[116:119], v[180:183], v[48:51], v[116:119]\n"
    "ds_read_b64_tr_b16 v[204:205], v247 offset:32768\n"
    "v_mfma_f32_16x16x32_bf16 v[124:127], v[180:183], v[52:55], v[124:127]\n"
    "ds_read_b64_tr_b16 v[206:207], v247 offset:33792\n"
    "s_waitcnt lgkmcnt(8)\n"
    "v_mfma_f32_16x16x32_bf16 v[128:131], v[184:187], v[48:51], v[128:131]\n"
    "ds_read_b64_tr_b16 v[176:177], v240 offset:40960\n"
    "v_mfma_f32_16x16x32_bf16 v[136:139], v[184:187], v[52:55], v[136:139]\n"
    "ds_read_b64_tr_b16 v[178:179], v240 offset:41984\n"
    "v_mfma_f32_16x16x32_bf16 v[132:135], v[188:191], v[48:51], v[132:135]\n"
    "ds_read_b64_tr_b16 v[180:181], v241 offset:40960\n"
    "v_mfma_f32_16x16x32_bf16 v[140:143], v[188:191], v[52:55], v[140:143]\n"
    "ds_read_b64_tr_b16 v[182:183], v241 offset:41984\n"
    "s_waitcnt lgkmcnt(8)\n"
    "v_mfma_f32_16x16x32_bf16 v[144:147], v[192:195], v[48:51], v[144:147]\n"
    "ds_read_b64_tr_b16 v[184:185], v242 offset:40960\n"
    "v_mfma_f32_16x16x32_bf16 v[152:155], v[192:195], v[52:55], v[152:155]\n"
    "ds_read_b64_tr_b16 v[186:187], v242 offset:41984\n"
    "v_mfma_f32_16x16x32_bf16 v[148:151], v[196:199], v[48:51], v[148:151]\n"
    "ds_read_b64_tr_b16 v[188:189], v243 offset:40960\n"
    "v_mfma_f32_16x16x32_bf16 v[156:159], v[196:199], v[52:55], v[156:159]\n"
    "ds_read_b64_tr_b16 v[190:191], v243 offset:41984\n"
    "s_waitcnt lgkmcnt(8)\n"
    "v_mfma_f32_16x16x32_bf16 v[160:163], v[200:203], v[48:51], v[160:163]\n"
    "ds_read_b64_tr_b16 v[192:193], v244 offset:40960\n"
    "v_mfma_f32_16x16x32_bf16 v[168:171], v[200:203], v[52:55], v[168:171]\n"
    "ds_read_b64_tr_b16 v[194:195], v244 offset:41984\n"
    "v_mfma_f32_16x16x32_bf16 v[164:167], v[204:207], v[48:51], v[164:167]\n"
    "ds_read_b64_tr_b16 v[196:197], v245 offset:40960\n"
    "v_mfma_f32_16x16x32_bf16 v[172:175], v[204:207], v[52:55], v[172:175]\n"
    "ds_read_b64_tr_b16 v[198:199], v245 offset:41984\n"
    "s_waitcnt lgkmcnt(8)\n"
    "v_mfma_f32_16x16x32_bf16 v[112:115], v[176:179], v[56:59], v[112:115]\n"
    "ds_read_b64_tr_b16 v[200:201], v246 offset:40960\n"
    "v_mfma_f32_16x16x32_bf16 v[120:123], v[176:179], v[60:63], v[120:123]\n"
    "ds_read_b64_tr_b16 v[202:203], v246 offset:41984\n"
    "v_mfma_f32_16x16x32_bf16 v[116:119], v[180:183], v[56:59], v[116:119]\n"
    "ds_read_b64_tr_b16 v[204:205], v247 offset:40960\n"
    "v_mfma_f32_16x16x32_bf16 v[124:127], v[180:183], v[60:63], v[124:127]\n"
    "ds_read_b64_tr_b16 v[206:207], v247 offset:41984\n"
    "s_waitcnt lgkmcnt(8)\n"
    "v_mfma_f32_16x16x32_bf16 v[128:131], v[184:187], v[56:59], v[128:131]\n"
    "ds_read_b64_tr_b16 v[176:177], v240 offset:49152\n"
    "v_mfma_f32_16x16x32_bf16 v[136:139], v[184:187], v[60:63], v[136:139]\n"
    "ds_read_b64_tr_b16 v[178:179], v240 offset:50176\n"
    "v_mfma_f32_16x16x32_bf16 v[132:135], v[188:191], v[56:59], v[132:135]\n"
    "ds_read_b64_tr_b16 v[180:181], v241 offset:49152\n"
    "v_mfma_f32_16x16x32_bf16 v[140:143], v[188:191], v[60:63], v[140:143]\n"
    "ds_read_b64_tr_b16 v[182:183], v241 offset:50176\n"
    "s_waitcnt lgkmcnt(8)\n"
    "v_mfma_f32_16x16x32_bf16 v[144:147], v[192:195], v[56:59], v[144:147]\n"
    "ds_read_b64_tr_b16 v[184:185], v242 offset:49152\n"
    "v_mfma_f32_16x16x32_bf16 v[152:155], v[192:195], v[60:63], v[152:155]\n"
    "ds_read_b64_tr_b16 v[186:187], v242 offset:50176\n"
    "v_mfma_f32_16x16x32_bf16 v[148:151], v[196:199], v[56:59], v[148:151]\n"
    "ds_read_b64_tr_b16 v[188:189], v243 offset:49152\n"
    "v_mfma_f32_16x16x32_bf16 v[156:159], v[196:199], v[60:63], v[156:159]\n"
    "ds_read_b64_tr_b16 v[190:191], v243 offset:50176\n"
    "s_waitcnt lgkmcnt(8)\n"
    "v_mfma_f32_16x16x32_bf16 v[160:163], v[200:203], v[56:59], v[160:163]\n"
    "ds_read_b64_tr_b16 v[192:193], v244 offset:49152\n"
    "v_mfma_f32_16x16x32_bf16 v[168:171], v[200:203], v[60:63], v[168:171]\n"
    "ds_read_b64_tr_b16 v[194:195], v244 offset:50176\n"
    "v_mfma_f32_16x16x32_bf16 v[164:167], v[204:207], v[56:59], v[164:167]\n"
    "ds_read_b64_tr_b16 v[196:197], v245 offset:49152\n"
    "v_mfma_f32_16x16x32_bf16 v[172:175], v[204:207], v[60:63], v[172:175]\n"
    "ds_read_b64_tr_b16 v[198:199], v245 offset:50176\n"
    "s_waitcnt vmcnt(0)\n"
    "s_mov_b64 exec, 1\n"
    "ds_add_u32 v249, v248 offset:28\n"
    "s_mov_b64 exec, -1\n"
    "s_waitcnt lgkmcnt(9)\n"
    "v_mfma_f32_16x16x32_bf16 v[112:115], v[176:179], v[64:67], v[112:115]\n"
    "ds_read_b64_tr_b16 v[200:201], v246 offset:49152\n"
    "v_mfma_f32_16x16x32_bf16 v[120:123], v[176:179], v[68:71], v[120:123]\n"
    "ds_read_b64_tr_b16 v[202:203], v246 offset:50176\n"
    "v_mfma_f32_16x16x32_bf16 v[116:119], v[180:183], v[64:67], v[116:119]\n"
    "ds_read_b64_tr_b16 v[204:205], v247 offset:49152\n"
    "v_mfma_f32_16x16x32_bf16 v[124:127], v[180:183], v[68:71], v[124:127]\n"
    "ds_read_b64_tr_b16 v[206:207], v247 offset:50176\n"
    "s_waitcnt lgkmcnt(9)\n"
    "v_mfma_f32_16x16x32_bf16 v[128:131], v[184:187], v[64:67], v[128:131]\n"
    "ds_read_b64_tr_b16 v[176:177], v240 offset:57344\n"
    "v_mfma_f32_16x16x32_bf16 v[136:139], v[184:187], v[68:71], v[136:139]\n"
    "ds_read_b64_tr_b16 v[178:179], v240 offset:58368\n"
    "v_mfma_f32_16x16x32_bf16 v[132:135], v[188:191], v[64:67], v[132:135]\n"
    "ds_read_b64_tr_b16 v[180:181], v241 offset:57344\n"
    "v_mfma_f32_16x16x32_bf16 v[140:143], v[188:191], v[68:71], v[140:143]\n"
    "ds_read_b64_tr_b16 v[182:183], v241 offset:58368\n"
    "s_waitcnt lgkmcnt(9)\n"
    "v_mfma_f32_16x16x32_bf16 v[144:147], v[192:195], v[64:67], v[144:147]\n"
    "ds_read_b64_tr_b16 v[184:185], v242 offset:57344\n"
    "v_mfma_f32_16x16x32_bf16 v[152:155], v[192:195], v[68:71], v[152:155]\n"
    "ds_read_b64_tr_b16 v[186:187], v242 offset:58368\n"
    "v_mfma_f32_16x16x32_bf16 v[148:151], v[196:199], v[64:67], v[148:151]\n"
    "ds_read_b64_tr_b16 v[188:189], v243 offset:57344\n"
    "v_mfma_f32_16x16x32_bf16 v[156:159], v[196:199], v[68:71], v[156:159]\n"
    "ds_read_b64_tr_b16 v[190:191], v243 offset:58368\n"
    "ds_read_b32 v229, v249 offset:28\n"
    "s_waitcnt lgkmcnt(9)\n"
    "v_mfma_f32_16x16x32_bf16 v[160:163], v[200:203], v[64:67], v[160:163]\n"
    "ds_read_b64_tr_b16 v[192:193], v244 offset:57344\n"
    "v_mfma_f32_16x16x32_bf16 v[168:171], v[200:203], v[68:71], v[168:171]\n"
    "ds_read_b64_tr_b16 v[194:195], v244 offset:58368\n"
    "v_mfma_f32_16x16x32_bf16 v[164:167], v[204:207], v[64:67], v[164:167]\n"
    "ds_read_b64_tr_b16 v[196:197], v245 offset:57344\n"
    "v_mfma_f32_16x16x32_bf16 v[172:175], v[204:207], v[68:71], v[172:175]\n"
    "ds_read_b64_tr_b16 v[198:199], v245 offset:58368\n"
    "s_waitcnt lgkmcnt(9)\n"
    "v_mfma_f32_16x16x32_bf16 v[112:115], v[176:179], v[72:75], v[112:115]\n"
    "ds_read_b64_tr_b16 v[200:201], v246 offset:57344\n"
    "v_mfma_f32_16x16x32_bf16 v[120:123], v[176:179], v[76:79], v[120:123]\n"
    "ds_read_b64_tr_b16 v[202:203], v246 offset:58368\n"
    "v_mfma_f32_16x16x32_bf16 v[116:119], v[180:183], v[72:75], v[116:119]\n"
    "ds_read_b64_tr_b16 v[204:205], v247 offset:57344\n"
    "v_mfma_f32_16x16x32_bf16 v[124:127], v[180:183], v[76:79], v[124:127]\n"
    "ds_read_b64_tr_b16 v[206:207], v247 offset:58368\n"
    "s_waitcnt lgkmcnt(8)\n"
    "v_readfirstlane_b32 s91, v229\n"
    "s_cmp_ge_u32 s91, 4\n"
    "s_cbranch_scc0 .Lgm_lc_12\n"
    ".Lgm_lcb_13:\n"
    "v_add_u32 v240, 0x10000, v240\n"
    "v_mfma_f32_16x16x32_bf16 v[128:131], v[184:187], v[72:75], v[128:131]\n"
    "ds_read_b64_tr_b16 v[176:177], v240 offset:0\n"
    "v_mfma_f32_16x16x32_bf16 v[136:139], v[184:187], v[76:79], v[136:139]\n"
    "ds_read_b64_tr_b16 v[178:179], v240 offset:1024\n"
    "v_add_u32 v241, 0x10000, v241\n"
    "v_mfma_f32_16x16x32_bf16 v[132:135], v[188:191], v[72:75], v[132:135]\n"
    "ds_read_b64_tr_b16 v[180:181], v241 offset:0\n"
    "v_mfma_f32_16x16x32_bf16 v[140:143], v[188:191], v[76:79], v[140:143]\n"
    "ds_read_b64_tr_b16 v[182:183], v241 offset:1024\n"
    "s_waitcnt lgkmcnt(8)\n"
    "v_add_u32 v242, 0x10000, v242\n"
    "v_mfma_f32_16x16x32_bf16 v[144:147], v[192:195], v[72:75], v[144:147]\n"
    "ds_read_b64_tr_b16 v[184:185], v242 offset:0\n"
    "v_mfma_f32_16x16x32_bf16 v[152:155], v[192:195], v[76:79], v[152:155]\n"
    "ds_read_b64_tr_b16 v[186:187], v242 offset:1024\n"
    "v_add_u32 v243, 0x10000, v243\n"
    "v_mfma_f32_16x16x32_bf16 v[148:151], v[196:199], v[72:75], v[148:151]\n"
    "ds_read_b64_tr_b16 v[188:189], v243 offset:0\n"
    "v_mfma_f32_16x16x32_bf16 v[156:159], v[196:199], v[76:79], v[156:159]\n"
    "ds_read_b64_tr_b16 v[190:191], v243 offset:1024\n"
    "s_waitcnt lgkmcnt(8)\n"
    "v_add_u32 v244, 0x10000, v244\n"
    "v_mfma_f32_16x16x32_bf16 v[160:163], v[200:203], v[72:75], v[160:163]\n"
    "ds_read_b64_tr_b16 v[192:193], v244 offset:0\n"
    "v_mfma_f32_16x16x32_bf16 v[168:171], v[200:203], v[76:79], v[168:171]\n"
    "ds_read_b64_tr_b16 v[194:195], v244 offset:1024\n"
    "v_add_u32 v245, 0x10000, v245\n"
    "v_mfma_f32_16x16x32_bf16 v[164:167], v[204:207], v[72:75], v[164:167]\n"
    "ds_read_b64_tr_b16 v[196:197], v245 offset:0\n"
    "v_mfma_f32_16x16x32_bf16 v[172:175], v[204:207], v[76:79], v[172:175]\n"
    "ds_read_b64_tr_b16 v[198:199], v245 offset:1024\n"
    "s_waitcnt lgkmcnt(8)\n"
    "v_add_u32 v246, 0x10000, v246\n"
    "v_mfma_f32_16x16x32_bf16 v[112:115], v[176:179], v[80:83], v[112:115]\n"
    "ds_read_b64_tr_b16 v[200:201], v246 offset:0\n"
    "v_mfma_f32_16x16x32_bf16 v[120:123], v[176:179], v[84:87], v[120:123]\n"
    "ds_read_b64_tr_b16 v[202:203], v246 offset:1024\n"
    "v_add_u32 v247, 0x10000, v247\n"
    "v_mfma_f32_16x16x32_bf16 v[116:119], v[180:183], v[80:83], v[116:119]\n"
    "ds_read_b64_tr_b16 v[204:205], v247 offset:0\n"
    "v_mfma_f32_16x16x32_bf16 v[124:127], v[180:183], v[84:87], v[124:127]\n"
    "ds_read_b64_tr_b16 v[206:207], v247 offset:1024\n"
    "s_waitcnt lgkmcnt(8)\n"
    "v_mfma_f32_16x16x32_bf16 v[128:131], v[184:187], v[80:83], v[128:131]\n"
    "ds_read_b64_tr_b16 v[176:177], v240 offset:8192\n"
    "v_mfma_f32_16x16x32_bf16 v[136:139], v[184:187], v[84:87], v[136:139]\n"
    "ds_read_b64_tr_b16 v[178:179], v240 offset:9216\n"
    "v_mfma_f32_16x16x32_bf16 v[132:135], v[188:191], v[80:83], v[132:135]\n"
    "ds_read_b64_tr_b16 v[180:181], v241 offset:8192\n"
    "v_mfma_f32_16x16x32_bf16 v[140:143], v[188:191], v[84:87], v[140:143]\n"
    "ds_read_b64_tr_b16 v[182:183], v241 offset:9216\n"
    "s_waitcnt lgkmcnt(8)\n"
    "v_mfma_f32_16x16x32_bf16 v[144:147], v[192:195], v[80:83], v[144:147]\n"
    "ds_read_b64_tr_b16 v[184:185], v242 offset:8192\n"
    "v_mfma_f32_16x16x32_bf16 v[152:155], v[192:195], v[84:87], v[152:155]\n"
    "ds_read_b64_tr_b16 v[186:187], v242 offset:9216\n"
    "v_mfma_f32_16x16x32_bf16 v[148:151], v[196:199], v[80:83], v[148:151]\n"
    "ds_read_b64_tr_b16 v[188:189], v243 offset:8192\n"
    "v_mfma_f32_16x16x32_bf16 v[156:159], v[196:199], v[84:87], v[156:159]\n"
    "ds_read_b64_tr_b16 v[190:191], v243 offset:9216\n"
    "s_waitcnt lgkmcnt(8)\n"
    "v_mfma_f32_16x16x32_bf16 v[160:163], v[200:203], v[80:83], v[160:163]\n"
    "ds_read_b64_tr_b16 v[192:193], v244 offset:8192\n"
    "v_mfma_f32_16x16x32_bf16 v[168:171], v[200:203], v[84:87], v[168:171]\n"
    "ds_read_b64_tr_b16 v[194:195], v244 offset:9216\n"
    "v_mfma_f32_16x16x32_bf16 v[164:167], v[204:207], v[80:83], v[164:167]\n"
    "ds_read_b64_tr_b16 v[196:197], v245 offset:8192\n"
    "v_mfma_f32_16x16x32_bf16 v[172:175], v[204:207], v[84:87], v[172:175]\n"
    "ds_read_b64_tr_b16 v[198:199], v245 offset:9216\n"
    "s_waitcnt lgkmcnt(8)\n"
    "v_mfma_f32_16x16x32_bf16 v[112:115], v[176:179], v[88:91], v[112:115]\n"
    "ds_read_b64_tr_b16 v[200:201], v246 offset:8192\n"
    "v_mfma_f32_16x16x32_bf16 v[120:123], v[176:179], v[92:95], v[120:123]\n"
    "ds_read_b64_tr_b16 v[202:203], v246 offset:9216\n"
    "v_mfma_f32_16x16x32_bf16 v[116:119], v[180:183], v[88:91], v[116:119]\n"
    "ds_read_b64_tr_b16 v[204:205], v247 offset:8192\n"
    "v_mfma_f32_16x16x32_bf16 v[124:127], v[180:183], v[92:95], v[124:127]\n"
    "ds_read_b64_tr_b16 v[206:207], v247 offset:9216\n"
    "s_waitcnt lgkmcnt(8)\n"
    "v_mfma_f32_16x16x32_bf16 v[128:131], v[184:187], v[88:91], v[128:131]\n"
    "ds_read_b64_tr_b16 v[176:177], v240 offset:16384\n"
    "v_mfma_f32_16x16x32_bf16 v[136:139], v[184:187], v[92:95], v[136:139]\n"
    "ds_read_b64_tr_b16 v[178:179], v240 offset:17408\n"
    "v_mfma_f32_16x16x32_bf16 v[132:135], v[188:191], v[88:91], v[132:135]\n"
    "ds_read_b64_tr_b16 v[180:181], v241 offset:16384\n"
    "v_mfma_f32_16x16x32_bf16 v[140:143], v[188:191], v[92:95], v[140:143]\n"
    "ds_read_b64_tr_b16 v[182:183], v241 offset:17408\n"
    "s_waitcnt lgkmcnt(8)\n"
    "v_mfma_f32_16x16x32_bf16 v[144:147], v[192:195], v[88:91], v[144:147]\n"
    "ds_read_b64_tr_b16 v[184:185], v242 offset:16384\n"
    "v_mfma_f32_16x16x32_bf16 v[152:155], v[192:195], v[92:95], v[152:155]\n"
    "ds_read_b64_tr_b16 v[186:187], v242 offset:17408\n"
    "v_mfma_f32_16x16x32_bf16 v[148:151], v[196:199], v[88:91], v[148:151]\n"
    "ds_read_b64_tr_b16 v[188:189], v243 offset:16384\n"
    "v_mfma_f32_16x16x32_bf16 v[156:159], v[196:199], v[92:95], v[156:159]\n"
    "ds_read_b64_tr_b16 v[190:191], v243 offset:17408\n"
    "s_waitcnt lgkmcnt(8)\n"
    "v_mfma_f32_16x16x32_bf16 v[160:163], v[200:203], v[88:91], v[160:163]\n"
    "ds_read_b64_tr_b16 v[192:193], v244 offset:16384\n"
    "v_mfma_f32_16x16x32_bf16 v[168:171], v[200:203], v[92:95], v[168:171]\n"
    "ds_read_b64_tr_b16 v[194:195], v244 offset:17408\n"
    "v_mfma_f32_16x16x32_bf16 v[164:167], v[204:207], v[88:91], v[164:167]\n"
    "ds_read_b64_tr_b16 v[196:197], v245 offset:16384\n"
    "v_mfma_f32_16x16x32_bf16 v[172:175], v[204:207], v[92:95], v[172:175]\n"
    "ds_read_b64_tr_b16 v[198:199], v245 offset:17408\n"
    "s_waitcnt lgkmcnt(8)\n"
    "v_mfma_f32_16x16x32_bf16 v[112:115], v[176:179], v[96:99], v[112:115]\n"
    "ds_read_b64_tr_b16 v[200:201], v246 offset:16384\n"
    "v_mfma_f32_16x16x32_bf16 v[120:123], v[176:179], v[100:103], v[120:123]\n"
    "ds_read_b64_tr_b16 v[202:203], v246 offset:17408\n"
    "v_mfma_f32_16x16x32_bf16 v[116:119], v[180:183], v[96:99], v[116:119]\n"
    "ds_read_b64_tr_b16 v[204:205], v247 offset:16384\n"
    "v_mfma_f32_16x16x32_bf16 v[124:127], v[180:183], v[100:103], v[124:127]\n"
    "ds_read_b64_tr_b16 v[206:207], v247 offset:17408\n"
    "s_waitcnt lgkmcnt(8)\n"
    "v_mfma_f32_16x16x32_bf16 v[128:131], v[184:187], v[96:99], v[128:131]\n"
    "ds_read_b64_tr_b16 v[176:177], v240 offset:24576\n"
    "v_mfma_f32_16x16x32_bf16 v[136:139], v[184:187], v[100:103], v[136:139]\n"
    "ds_read_b64_tr_b16 v[178:179], v240 offset:25600\n"
    "v_mfma_f32_16x16x32_bf16 v[132:135], v[188:191], v[96:99], v[132:135]\n"
    "ds_read_b64_tr_b16 v[180:181], v241 offset:24576\n"
    "v_mfma_f32_16x16x32_bf16 v[140:143], v[188:191], v[100:103], v[140:143]\n"
    "ds_read_b64_tr_b16 v[182:183], v241 offset:25600\n"
    "s_waitcnt lgkmcnt(8)\n"
    "v_mfma_f32_16x16x32_bf16 v[144:147], v[192:195], v[96:99], v[144:147]\n"
    "ds_read_b64_tr_b16 v[184:185], v242 offset:24576\n"
    "v_mfma_f32_16x16x32_bf16 v[152:155], v[192:195], v[100:103], v[152:155]\n"
    "ds_read_b64_tr_b16 v[186:187], v242 offset:25600\n"
    "v_mfma_f32_16x16x32_bf16 v[148:151], v[196:199], v[96:99], v[148:151]\n"
    "ds_read_b64_tr_b16 v[188:189], v243 offset:24576\n"
    "v_mfma_f32_16x16x32_bf16 v[156:159], v[196:199], v[100:103], v[156:159]\n"
    "ds_read_b64_tr_b16 v[190:191], v243 offset:25600\n"
    "s_waitcnt lgkmcnt(8)\n"
    "v_mfma_f32_16x16x32_bf16 v[160:163], v[200:203], v[96:99], v[160:163]\n"
    "ds_read_b64_tr_b16 v[192:193], v244 offset:24576\n"
    "v_mfma_f32_16x16x32_bf16 v[168:171], v[200:203], v[100:103], v[168:171]\n"
    "ds_read_b64_tr_b16 v[194:195], v244 offset:25600\n"
    "v_mfma_f32_16x16x32_bf16 v[164:167], v[204:207], v[96:99], v[164:167]\n"
    "ds_read_b64_tr_b16 v[196:197], v245 offset:24576\n"
    "v_mfma_f32_16x16x32_bf16 v[172:175], v[204:207], v[100:103], v[172:175]\n"
    "ds_read_b64_tr_b16 v[198:199], v245 offset:25600\n"
    "s_waitcnt lgkmcnt(8)\n"
    "v_mfma_f32_16x16x32_bf16 v[112:115], v[176:179], v[104:107], v[112:115]\n"
    "ds_read_b64_tr_b16 v[200:201], v246 offset:24576\n"
    "v_mfma_f32_16x16x32_bf16 v[120:123], v[176:179], v[108:111], v[120:123]\n"
    "ds_read_b64_tr_b16 v[202:203], v246 offset:25600\n"
    "v_mfma_f32_16x16x32_bf16 v[116:119], v[180:183], v[104:107], v[116:119]\n"
    "ds_read_b64_tr_b16 v[204:205], v247 offset:24576\n"
    "v_mfma_f32_16x16x32_bf16 v[124:127], v[180:183], v[108:111], v[124:127]\n"
    "ds_read_b64_tr_b16 v[206:207], v247 offset:25600\n"
    "s_waitcnt lgkmcnt(8)\n"
    "v_mfma_f32_16x16x32_bf16 v[128:131], v[184:187], v[104:107], v[128:131]\n"
    "v_mfma_f32_16x16x32_bf16 v[136:139], v[184:187], v[108:111], v[136:139]\n"
    "v_mfma_f32_16x16x32_bf16 v[132:135], v[188:191], v[104:107], v[132:135]\n"
    "v_mfma_f32_16x16x32_bf16 v[140:143], v[188:191], v[108:111], v[140:143]\n"
    "s_waitcnt lgkmcnt(6)\n"
    "v_mfma_f32_16x16x32_bf16 v[144:147], v[192:195], v[104:107], v[144:147]\n"
    "v_mfma_f32_16x16x32_bf16 v[152:155], v[192:195], v[108:111], v[152:155]\n"
    "s_waitcnt lgkmcnt(4)\n"
    "v_mfma_f32_16x16x32_bf16 v[148:151], v[196:199], v[104:107], v[148:151]\n"
    "v_mfma_f32_16x16x32_bf16 v[156:159], v[196:199], v[108:111], v[156:159]\n"
    "s_waitcnt lgkmcnt(2)\n"
    "v_mfma_f32_16x16x32_bf16 v[160:163], v[200:203], v[104:107], v[160:163]\n"
    "v_mfma_f32_16x16x32_bf16 v[168:171], v[200:203], v[108:111], v[168:171]\n"
    "s_waitcnt lgkmcnt(0)\n"
    "v_mfma_f32_16x16x32_bf16 v[164:167], v[204:207], v[104:107], v[164:167]\n"
    "v_mfma_f32_16x16x32_bf16 v[172:175], v[204:207], v[108:111], v[172:175]\n"
    "s_mov_b64 exec, 1\n"
    "ds_add_u32 v249, v248 offset:12\n"
    "s_mov_b64 exec, -1\n"
    "v_pk_fma_f32 v[208:209], v[112:113], s[82:83], -0.5 op_sel_hi:[1,0,0]\n"
    "v_pk_fma_f32 v[210:211], v[114:115], s[82:83], -0.5 op_sel_hi:[1,0,0]\n"
    "v_pk_fma_f32 v[212:213], v[116:117], s[82:83], -0.5 op_sel_hi:[1,0,0]\n"
    "v_pk_fma_f32 v[214:215], v[118:119], s[82:83], -0.5 op_sel_hi:[1,0,0]\n"
    "v_exp_f32 v208, v208\n"
    "v_exp_f32 v209, v209\n"
    "v_exp_f32 v210, v210\n"
    "v_exp_f32 v211, v211\n"
    "v_exp_f32 v212, v212\n"
    "v_exp_f32 v213, v213\n"
    "v_exp_f32 v214, v214\n"
    "v_exp_f32 v215, v215\n"
    "v_pk_add_f32 v[216:217], v[208:209], s[82:83] op_sel:[0,1] op_sel_hi:[1,1]\n"
    "v_pk_add_f32 v[218:219], v[210:211], s[82:83] op_sel:[0,1] op_sel_hi:[1,1]\n"
    "v_pk_add_f32 v[220:221], v[212:213], s[82:83] op_sel:[0,1] op_sel_hi:[1,1]\n"
    "v_pk_add_f32 v[222:223], v[214:215], s[82:83] op_sel:[0,1] op_sel_hi:[1,1]\n"
    "v_pk_fma_f32 v[208:209], v[208:209], v[216:217], 1.0 op_sel_hi:[1,1,0]\n"
    "v_pk_fma_f32 v[210:211], v[210:211], v[218:219], 1.0 op_sel_hi:[1,1,0]\n"
    "v_pk_fma_f32 v[212:213], v[212:213], v[220:221], 1.0 op_sel_hi:[1,1,0]\n"
    "v_pk_fma_f32 v[214:215], v[214:215], v[222:223], 1.0 op_sel_hi:[1,1,0]\n"
    "v_rcp_f32 v208, v208\n"
    "v_rcp_f32 v209, v209\n"
    "v_rcp_f32 v210, v210\n"
    "v_rcp_f32 v211, v211\n"
    "v_rcp_f32 v212, v212\n"
    "v_rcp_f32 v213, v213\n"
    "v_rcp_f32 v214, v214\n"
    "v_rcp_f32 v215, v215\n"
    "v_pk_fma_f32 v[208:209], v[112:113], v[208:209], v[112:113] neg_lo:[1,0,0] neg_hi:[1,0,0]\n"
    "v_pk_fma_f32 v[210:211], v[114:115], v[210:211], v[114:115] neg_lo:[1,0,0] neg_hi:[1,0,0]\n"
    "v_pk_fma_f32 v[212:213], v[116:117], v[212:213], v[116:117] neg_lo:[1,0,0] neg_hi:[1,0,0]\n"
    "v_pk_fma_f32 v[214:215], v[118:119], v[214:215], v[118:119] neg_lo:[1,0,0] neg_hi:[1,0,0]\n"
    "v_cvt_pk_bf16_f32 v0, v208, v209\n"
    "v_cvt_pk_bf16_f32 v1, v210, v211\n"
    "v_cvt_pk_bf16_f32 v2, v212, v213\n"
    "v_cvt_pk_bf16_f32 v3, v214, v215\n"
    "buffer_store_dwordx4 v[0:3], v239, s[24:27], s50 offen offset:0 sc1\n"
    "v_pk_fma_f32 v[208:209], v[120:121], s[82:83], -0.5 op_sel_hi:[1,0,0]\n"
    "v_pk_fma_f32 v[210:211], v[122:123], s[82:83], -0.5 op_sel_hi:[1,0,0]\n"
    "v_pk_fma_f32 v[212:213], v[124:125], s[82:83], -0.5 op_sel_hi:[1,0,0]\n"
    "v_pk_fma_f32 v[214:215], v[126:127], s[82:83], -0.5 op_sel_hi:[1,0,0]\n"
    "v_exp_f32 v208, v208\n"
    "v_exp_f32 v209, v209\n"
    "v_exp_f32 v210, v210\n"
    "v_exp_f32 v211, v211\n"
    "v_exp_f32 v212, v212\n"
    "v_exp_f32 v213, v213\n"
    "v_exp_f32 v214, v214\n"
    "v_exp_f32 v215, v215\n"
    "v_pk_add_f32 v[216:217], v[208:209], s[82:83] op_sel:[0,1] op_sel_hi:[1,1]\n"
    "v_pk_add_f32 v[218:219], v[210:211], s[82:83] op_sel:[0,1] op_sel_hi:[1,1]\n"
    "v_pk_add_f32 v[220:221], v[212:213], s[82:83] op_sel:[0,1] op_sel_hi:[1,1]\n"
    "v_pk_add_f32 v[222:223], v[214:215], s[82:83] op_sel:[0,1] op_sel_hi:[1,1]\n"
    "v_pk_fma_f32 v[208:209], v[208:209], v[216:217], 1.0 op_sel_hi:[1,1,0]\n"
    "v_pk_fma_f32 v[210:211], v[210:211], v[218:219], 1.0 op_sel_hi:[1,1,0]\n"
    "v_pk_fma_f32 v[212:213], v[212:213], v[220:221], 1.0 op_sel_hi:[1,1,0]\n"
    "v_pk_fma_f32 v[214:215], v[214:215], v[222:223], 1.0 op_sel_hi:[1,1,0]\n"
    "v_rcp_f32 v208, v208\n"
    "v_rcp_f32 v209, v209\n"
    "v_rcp_f32 v210, v210\n"
    "v_rcp_f32 v211, v211\n"
    "v_rcp_f32 v212, v212\n"
    "v_rcp_f32 v213, v213\n"
    "v_rcp_f32 v214, v214\n"
    "v_rcp_f32 v215, v215\n"
    "v_pk_fma_f32 v[208:209], v[120:121], v[208:209], v[120:121] neg_lo:[1,0,0] neg_hi:[1,0,0]\n"
    "v_pk_fma_f32 v[210:211], v[122:123], v[210:211], v[122:123] neg_lo:[1,0,0] neg_hi:[1,0,0]\n"
    "v_pk_fma_f32 v[212:213], v[124:125], v[212:213], v[124:125] neg_lo:[1,0,0] neg_hi:[1,0,0]\n"
    "v_pk_fma_f32 v[214:215], v[126:127], v[214:215], v[126:127] neg_lo:[1,0,0] neg_hi:[1,0,0]\n"
    "v_cvt_pk_bf16_f32 v4, v208, v209\n"
    "v_cvt_pk_bf16_f32 v5, v210, v211\n"
    "v_cvt_pk_bf16_f32 v6, v212, v213\n"
    "v_cvt_pk_bf16_f32 v7, v214, v215\n"
    "buffer_store_dwordx4 v[4:7], v239, s[24:27], s50 offen offset:256 sc1\n"
    "v_pk_fma_f32 v[208:209], v[128:129], s[82:83], -0.5 op_sel_hi:[1,0,0]\n"
    "v_pk_fma_f32 v[210:211], v[130:131], s[82:83], -0.5 op_sel_hi:[1,0,0]\n"
    "v_pk_fma_f32 v[212:213], v[132:133], s[82:83], -0.5 op_sel_hi:[1,0,0]\n"
    "v_pk_fma_f32 v[214:215], v[134:135], s[82:83], -0.5 op_sel_hi:[1,0,0]\n"
    "v_exp_f32 v208, v208\n"
    "v_exp_f32 v209, v209\n"
    "v_exp_f32 v210, v210\n"
    "v_exp_f32 v211, v211\n"
    "v_exp_f32 v212, v212\n"
    "v_exp_f32 v213, v213\n"
    "v_exp_f32 v214, v214\n"
    "v_exp_f32 v215, v215\n"
    "v_pk_add_f32 v[216:217], v[208:209], s[82:83] op_sel:[0,1] op_sel_hi:[1,1]\n"
    "v_pk_add_f32 v[218:219], v[210:211], s[82:83] op_sel:[0,1] op_sel_hi:[1,1]\n"
    "v_pk_add_f32 v[220:221], v[212:213], s[82:83] op_sel:[0,1] op_sel_hi:[1,1]\n"
    "v_pk_add_f32 v[222:223], v[214:215], s[82:83] op_sel:[0,1] op_sel_hi:[1,1]\n"
    "v_pk_fma_f32 v[208:209], v[208:209], v[216:217], 1.0 op_sel_hi:[1,1,0]\n"
    "v_pk_fma_f32 v[210:211], v[210:211], v[218:219], 1.0 op_sel_hi:[1,1,0]\n"
    "v_pk_fma_f32 v[212:213], v[212:213], v[220:221], 1.0 op_sel_hi:[1,1,0]\n"
    "v_pk_fma_f32 v[214:215], v[214:215], v[222:223], 1.0 op_sel_hi:[1,1,0]\n"
    "v_rcp_f32 v208, v208\n"
    "v_rcp_f32 v209, v209\n"
    "v_rcp_f32 v210, v210\n"
    "v_rcp_f32 v211, v211\n"
    "v_rcp_f32 v212, v212\n"
    "v_rcp_f32 v213, v213\n"
    "v_rcp_f32 v214, v214\n"
    "v_rcp_f32 v215, v215\n"
    "v_pk_fma_f32 v[208:209], v[128:129], v[208:209], v[128:129] neg_lo:[1,0,0] neg_hi:[1,0,0]\n"
    "v_pk_fma_f32 v[210:211], v[130:131], v[210:211], v[130:131] neg_lo:[1,0,0] neg_hi:[1,0,0]\n"
    "v_pk_fma_f32 v[212:213], v[132:133], v[212:213], v[132:133] neg_lo:[1,0,0] neg_hi:[1,0,0]\n"
    "v_pk_fma_f32 v[214:215], v[134:135], v[214:215], v[134:135] neg_lo:[1,0,0] neg_hi:[1,0,0]\n"
    "v_cvt_pk_bf16_f32 v8, v208, v209\n"
    "v_cvt_pk_bf16_f32 v9, v210, v211\n"
    "v_cvt_pk_bf16_f32 v10, v212, v213\n"
    "v_cvt_pk_bf16_f32 v11, v214, v215\n"
    "buffer_store_dwordx4 v[8:11], v239, s[24:27], s50 offen offset:2048 sc1\n"
    "v_pk_fma_f32 v[208:209], v[136:137], s[82:83], -0.5 op_sel_hi:[1,0,0]\n"
    "v_pk_fma_f32 v[210:211], v[138:139], s[82:83], -0.5 op_sel_hi:[1,0,0]\n"
    "v_pk_fma_f32 v[212:213], v[140:141], s[82:83], -0.5 op_sel_hi:[1,0,0]\n"
    "v_pk_fma_f32 v[214:215], v[142:143], s[82:83], -0.5 op_sel_hi:[1,0,0]\n"
    "v_exp_f32 v208, v208\n"
    "v_exp_f32 v209, v209\n"
    "v_exp_f32 v210, v210\n"
    "v_exp_f32 v211, v211\n"
    "v_exp_f32 v212, v212\n"
    "v_exp_f32 v213, v213\n"
    "v_exp_f32 v214, v214\n"
    "v_exp_f32 v215, v215\n"
    "v_pk_add_f32 v[216:217], v[208:209], s[82:83] op_sel:[0,1] op_sel_hi:[1,1]\n"
    "v_pk_add_f32 v[218:219], v[210:211], s[82:83] op_sel:[0,1] op_sel_hi:[1,1]\n"
    "v_pk_add_f32 v[220:221], v[212:213], s[82:83] op_sel:[0,1] op_sel_hi:[1,1]\n"
    "v_pk_add_f32 v[222:223], v[214:215], s[82:83] op_sel:[0,1] op_sel_hi:[1,1]\n"
    "v_pk_fma_f32 v[208:209], v[208:209], v[216:217], 1.0 op_sel_hi:[1,1,0]\n"
    "v_pk_fma_f32 v[210:211], v[210:211], v[218:219], 1.0 op_sel_hi:[1,1,0]\n"
    "v_pk_fma_f32 v[212:213], v[212:213], v[220:221], 1.0 op_sel_hi:[1,1,0]\n"
    "v_pk_fma_f32 v[214:215], v[214:215], v[222:223], 1.0 op_sel_hi:[1,1,0]\n"
    "v_rcp_f32 v208, v208\n"
    "v_rcp_f32 v209, v209\n"
    "v_rcp_f32 v210, v210\n"
    "v_rcp_f32 v211, v211\n"
    "v_rcp_f32 v212, v212\n"
    "v_rcp_f32 v213, v213\n"
    "v_rcp_f32 v214, v214\n"
    "v_rcp_f32 v215, v215\n"
    "v_pk_fma_f32 v[208:209], v[136:137], v[208:209], v[136:137] neg_lo:[1,0,0] neg_hi:[1,0,0]\n"
    "v_pk_fma_f32 v[210:211], v[138:139], v[210:211], v[138:139] neg_lo:[1,0,0] neg_hi:[1,0,0]\n"
    "v_pk_fma_f32 v[212:213], v[140:141], v[212:213], v[140:141] neg_lo:[1,0,0] neg_hi:[1,0,0]\n"
    "v_pk_fma_f32 v[214:215], v[142:143], v[214:215], v[142:143] neg_lo:[1,0,0] neg_hi:[1,0,0]\n"
    "v_cvt_pk_bf16_f32 v12, v208, v209\n"
    "v_cvt_pk_bf16_f32 v13, v210, v211\n"
    "v_cvt_pk_bf16_f32 v14, v212, v213\n"
    "v_cvt_pk_bf16_f32 v15, v214, v215\n"
    "buffer_store_dwordx4 v[12:15], v239, s[24:27], s50 offen offset:2304 sc1\n"
    "v_pk_fma_f32 v[208:209], v[144:145], s[82:83], -0.5 op_sel_hi:[1,0,0]\n"
    "v_pk_fma_f32 v[210:211], v[146:147], s[82:83], -0.5 op_sel_hi:[1,0,0]\n"
    "v_pk_fma_f32 v[212:213], v[148:149], s[82:83], -0.5 op_sel_hi:[1,0,0]\n"
    "v_pk_fma_f32 v[214:215], v[150:151], s[82:83], -0.5 op_sel_hi:[1,0,0]\n"
    "v_exp_f32 v208, v208\n"
    "v_exp_f32 v209, v209\n"
    "v_exp_f32 v210, v210\n"
    "v_exp_f32 v211, v211\n"
    "v_exp_f32 v212, v212\n"
    "v_exp_f32 v213, v213\n"
    "v_exp_f32 v214, v214\n"
    "v_exp_f32 v215, v215\n"
    "v_pk_add_f32 v[216:217], v[208:209], s[82:83] op_sel:[0,1] op_sel_hi:[1,1]\n"
    "v_pk_add_f32 v[218:219], v[210:211], s[82:83] op_sel:[0,1] op_sel_hi:[1,1]\n"
    "v_pk_add_f32 v[220:221], v[212:213], s[82:83] op_sel:[0,1] op_sel_hi:[1,1]\n"
    "v_pk_add_f32 v[222:223], v[214:215], s[82:83] op_sel:[0,1] op_sel_hi:[1,1]\n"
    "v_pk_fma_f32 v[208:209], v[208:209], v[216:217], 1.0 op_sel_hi:[1,1,0]\n"
    "v_pk_fma_f32 v[210:211], v[210:211], v[218:219], 1.0 op_sel_hi:[1,1,0]\n"
    "v_pk_fma_f32 v[212:213], v[212:213], v[220:221], 1.0 op_sel_hi:[1,1,0]\n"
    "v_pk_fma_f32 v[214:215], v[214:215], v[222:223], 1.0 op_sel_hi:[1,1,0]\n"
    "v_rcp_f32 v208, v208\n"
    "v_rcp_f32 v209, v209\n"
    "v_rcp_f32 v210, v210\n"
    "v_rcp_f32 v211, v211\n"
    "v_rcp_f32 v212, v212\n"
    "v_rcp_f32 v213, v213\n"
    "v_rcp_f32 v214, v214\n"
    "v_rcp_f32 v215, v215\n"
    "v_pk_fma_f32 v[208:209], v[144:145], v[208:209], v[144:145] neg_lo:[1,0,0] neg_hi:[1,0,0]\n"
    "v_pk_fma_f32 v[210:211], v[146:147], v[210:211], v[146:147] neg_lo:[1,0,0] neg_hi:[1,0,0]\n"
    "v_pk_fma_f32 v[212:213], v[148:149], v[212:213], v[148:149] neg_lo:[1,0,0] neg_hi:[1,0,0]\n"
    "v_pk_fma_f32 v[214:215], v[150:151], v[214:215], v[150:151] neg_lo:[1,0,0] neg_hi:[1,0,0]\n"
    "v_cvt_pk_bf16_f32 v16, v208, v209\n"
    "v_cvt_pk_bf16_f32 v17, v210, v211\n"
    "v_cvt_pk_bf16_f32 v18, v212, v213\n"
    "v_cvt_pk_bf16_f32 v19, v214, v215\n"
    "buffer_store_dwordx4 v[16:19], v239, s[24:27], s51 offen offset:0 sc1\n"
    "v_pk_fma_f32 v[208:209], v[152:153], s[82:83], -0.5 op_sel_hi:[1,0,0]\n"
    "v_pk_fma_f32 v[210:211], v[154:155], s[82:83], -0.5 op_sel_hi:[1,0,0]\n"
    "v_pk_fma_f32 v[212:213], v[156:157], s[82:83], -0.5 op_sel_hi:[1,0,0]\n"
    "v_pk_fma_f32 v[214:215], v[158:159], s[82:83], -0.5 op_sel_hi:[1,0,0]\n"
    "v_exp_f32 v208, v208\n"
    "v_exp_f32 v209, v209\n"
    "v_exp_f32 v210, v210\n"
    "v_exp_f32 v211, v211\n"
    "v_exp_f32 v212, v212\n"
    "v_exp_f32 v213, v213\n"
    "v_exp_f32 v214, v214\n"
    "v_exp_f32 v215, v215\n"
    "v_pk_add_f32 v[216:217], v[208:209], s[82:83] op_sel:[0,1] op_sel_hi:[1,1]\n"
    "v_pk_add_f32 v[218:219], v[210:211], s[82:83] op_sel:[0,1] op_sel_hi:[1,1]\n"
    "v_pk_add_f32 v[220:221], v[212:213], s[82:83] op_sel:[0,1] op_sel_hi:[1,1]\n"
    "v_pk_add_f32 v[222:223], v[214:215], s[82:83] op_sel:[0,1] op_sel_hi:[1,1]\n"
    "v_pk_fma_f32 v[208:209], v[208:209], v[216:217], 1.0 op_sel_hi:[1,1,0]\n"
    "v_pk_fma_f32 v[210:211], v[210:211], v[218:219], 1.0 op_sel_hi:[1,1,0]\n"
    "v_pk_fma_f32 v[212:213], v[212:213], v[220:221], 1.0 op_sel_hi:[1,1,0]\n"
    "v_pk_fma_f32 v[214:215], v[214:215], v[222:223], 1.0 op_sel_hi:[1,1,0]\n"
    "v_rcp_f32 v208, v208\n"
    "v_rcp_f32 v209, v209\n"
    "v_rcp_f32 v210, v210\n"
    "v_rcp_f32 v211, v211\n"
    "v_rcp_f32 v212, v212\n"
    "v_rcp_f32 v213, v213\n"
    "v_rcp_f32 v214, v214\n"
    "v_rcp_f32 v215, v215\n"
    "v_pk_fma_f32 v[208:209], v[152:153], v[208:209], v[152:153] neg_lo:[1,0,0] neg_hi:[1,0,0]\n"
    "v_pk_fma_f32 v[210:211], v[154:155], v[210:211], v[154:155] neg_lo:[1,0,0] neg_hi:[1,0,0]\n"
    "v_pk_fma_f32 v[212:213], v[156:157], v[212:213], v[156:157] neg_lo:[1,0,0] neg_hi:[1,0,0]\n"
    "v_pk_fma_f32 v[214:215], v[158:159], v[214:215], v[158:159] neg_lo:[1,0,0] neg_hi:[1,0,0]\n"
    "v_cvt_pk_bf16_f32 v20, v208, v209\n"
    "v_cvt_pk_bf16_f32 v21, v210, v211\n"
    "v_cvt_pk_bf16_f32 v22, v212, v213\n"
    "v_cvt_pk_bf16_f32 v23, v214, v215\n"
    "buffer_store_dwordx4 v[20:23], v239, s[24:27], s51 offen offset:256 sc1\n"
    "v_pk_fma_f32 v[208:209], v[160:161], s[82:83], -0.5 op_sel_hi:[1,0,0]\n"
    "v_pk_fma_f32 v[210:211], v[162:163], s[82:83], -0.5 op_sel_hi:[1,0,0]\n"
    "v_pk_fma_f32 v[212:213], v[164:165], s[82:83], -0.5 op_sel_hi:[1,0,0]\n"
    "v_pk_fma_f32 v[214:215], v[166:167], s[82:83], -0.5 op_sel_hi:[1,0,0]\n"
    "v_exp_f32 v208, v208\n"
    "v_exp_f32 v209, v209\n"
    "v_exp_f32 v210, v210\n"
    "v_exp_f32 v211, v211\n"
    "v_exp_f32 v212, v212\n"
    "v_exp_f32 v213, v213\n"
    "v_exp_f32 v214, v214\n"
    "v_exp_f32 v215, v215\n"
    "v_pk_add_f32 v[216:217], v[208:209], s[82:83] op_sel:[0,1] op_sel_hi:[1,1]\n"
    "v_pk_add_f32 v[218:219], v[210:211], s[82:83] op_sel:[0,1] op_sel_hi:[1,1]\n"
    "v_pk_add_f32 v[220:221], v[212:213], s[82:83] op_sel:[0,1] op_sel_hi:[1,1]\n"
    "v_pk_add_f32 v[222:223], v[214:215], s[82:83] op_sel:[0,1] op_sel_hi:[1,1]\n"
    "v_pk_fma_f32 v[208:209], v[208:209], v[216:217], 1.0 op_sel_hi:[1,1,0]\n"
    "v_pk_fma_f32 v[210:211], v[210:211], v[218:219], 1.0 op_sel_hi:[1,1,0]\n"
    "v_pk_fma_f32 v[212:213], v[212:213], v[220:221], 1.0 op_sel_hi:[1,1,0]\n"
    "v_pk_fma_f32 v[214:215], v[214:215], v[222:223], 1.0 op_sel_hi:[1,1,0]\n"
    "v_rcp_f32 v208, v208\n"
    "v_rcp_f32 v209, v209\n"
    "v_rcp_f32 v210, v210\n"
    "v_rcp_f32 v211, v211\n"
    "v_rcp_f32 v212, v212\n"
    "v_rcp_f32 v213, v213\n"
    "v_rcp_f32 v214, v214\n"
    "v_rcp_f32 v215, v215\n"
    "v_pk_fma_f32 v[208:209], v[160:161], v[208:209], v[160:161] neg_lo:[1,0,0] neg_hi:[1,0,0]\n"
    "v_pk_fma_f32 v[210:211], v[162:163], v[210:211], v[162:163] neg_lo:[1,0,0] neg_hi:[1,0,0]\n"
    "v_pk_fma_f32 v[212:213], v[164:165], v[212:213], v[164:165] neg_lo:[1,0,0] neg_hi:[1,0,0]\n"
    "v_pk_fma_f32 v[214:215], v[166:167], v[214:215], v[166:167] neg_lo:[1,0,0] neg_hi:[1,0,0]\n"
    "v_cvt_pk_bf16_f32 v24, v208, v209\n"
    "v_cvt_pk_bf16_f32 v25, v210, v211\n"
    "v_cvt_pk_bf16_f32 v26, v212, v213\n"
    "v_cvt_pk_bf16_f32 v27, v214, v215\n"
    "buffer_store_dwordx4 v[24:27], v239, s[24:27], s51 offen offset:2048 sc1\n"
    "v_pk_fma_f32 v[208:209], v[168:169], s[82:83], -0.5 op_sel_hi:[1,0,0]\n"
    "v_pk_fma_f32 v[210:211], v[170:171], s[82:83], -0.5 op_sel_hi:[1,0,0]\n"
    "v_pk_fma_f32 v[212:213], v[172:173], s[82:83], -0.5 op_sel_hi:[1,0,0]\n"
    "v_pk_fma_f32 v[214:215], v[174:175], s[82:83], -0.5 op_sel_hi:[1,0,0]\n"
    "v_exp_f32 v208, v208\n"
    "v_exp_f32 v209, v209\n"
    "v_exp_f32 v210, v210\n"
    "v_exp_f32 v211, v211\n"
    "v_exp_f32 v212, v212\n"
    "v_exp_f32 v213, v213\n"
    "v_exp_f32 v214, v214\n"
    "v_exp_f32 v215, v215\n"
    "v_pk_add_f32 v[216:217], v[208:209], s[82:83] op_sel:[0,1] op_sel_hi:[1,1]\n"
    "v_pk_add_f32 v[218:219], v[210:211], s[82:83] op_sel:[0,1] op_sel_hi:[1,1]\n"
    "v_pk_add_f32 v[220:221], v[212:213], s[82:83] op_sel:[0,1] op_sel_hi:[1,1]\n"
    "v_pk_add_f32 v[222:223], v[214:215], s[82:83] op_sel:[0,1] op_sel_hi:[1,1]\n"
    "v_pk_fma_f32 v[208:209], v[208:209], v[216:217], 1.0 op_sel_hi:[1,1,0]\n"
    "v_pk_fma_f32 v[210:211], v[210:211], v[218:219], 1.0 op_sel_hi:[1,1,0]\n"
    "v_pk_fma_f32 v[212:213], v[212:213], v[220:221], 1.0 op_sel_hi:[1,1,0]\n"
    "v_pk_fma_f32 v[214:215], v[214:215], v[222:223], 1.0 op_sel_hi:[1,1,0]\n"
    "v_rcp_f32 v208, v208\n"
    "v_rcp_f32 v209, v209\n"
    "v_rcp_f32 v210, v210\n"
    "v_rcp_f32 v211, v211\n"
    "v_rcp_f32 v212, v212\n"
    "v_rcp_f32 v213, v213\n"
    "v_rcp_f32 v214, v214\n"
    "v_rcp_f32 v215, v215\n"
    "v_pk_fma_f32 v[208:209], v[168:169], v[208:209], v[168:169] neg_lo:[1,0,0] neg_hi:[1,0,0]\n"
    "v_pk_fma_f32 v[210:211], v[170:171], v[210:211], v[170:171] neg_lo:[1,0,0] neg_hi:[1,0,0]\n"
    "v_pk_fma_f32 v[212:213], v[172:173], v[212:213], v[172:173] neg_lo:[1,0,0] neg_hi:[1,0,0]\n"
    "v_pk_fma_f32 v[214:215], v[174:175], v[214:215], v[174:175] neg_lo:[1,0,0] neg_hi:[1,0,0]\n"
    "v_cvt_pk_bf16_f32 v28, v208, v209\n"
    "v_cvt_pk_bf16_f32 v29, v210, v211\n"
    "v_cvt_pk_bf16_f32 v30, v212, v213\n"
    "v_cvt_pk_bf16_f32 v31, v214, v215\n"
    "buffer_store_dwordx4 v[28:31], v239, s[24:27], s51 offen offset:2304 sc1\n"
    "s_mul_i32 s53, s59, 24576\n"
    "s_add_u32 s53, s53, 57344\n"
    "s_and_b32 s97, s67, 3\n"
    "s_lshl_b32 s96, s97, 1\n"
    "s_add_u32 s96, s96, s68\n"
    "s_lshl_b32 s96, s96, 1\n"
    "s_and_b32 s85, s45, 3\n"
    "s_add_u32 s96, s96, s85\n"
    "s_cmp_ge_u32 s96, 14\n"
    "s_cselect_b32 s85, 14, 0\n"
    "s_sub_u32 s96, s96, s85\n"
    "s_mul_i32 s94, s59, 401408\n"
    "s_lshl_b32 s85, s68, 7\n"
    "s_add_u32 s94, s94, s85\n"
    "s_sub_u32 s94, s94, 14336\n"
    "s_mul_i32 s58, s96, 28672\n"
    "s_add_u32 s58, s58, s94\n"
    "s_add_u32 s98, s94, 401408\n"
    "s_mul_i32 s95, s67, 229376\n"
    "s_mul_i32 s84, s59, 28672\n"
    "s_add_u32 s95, s95, s84\n"
    "s_add_u32 s95, s95, 5767168\n"
    "s_lshl_b32 s72, s96, 11\n"
    "s_add_u32 s72, s72, s95\n"
    "s_add_u32 s99, s95, 28672\n"
    "v_mov_b32 v244, v239\n"
    "v_add_u32 v245, 114688, v239\n"
    "v_add_u32 v252, s53, v252\n"
    "v_lshrrev_b32 v22, 2, v255\n"
    "v_lshlrev_b32 v22, 7, v22\n"
    "v_and_b32 v23, 24, v255\n"
    "v_lshl_add_u32 v22, v23, 2, v22\n"
    "v_and_b32 v23, 3, v255\n"
    "v_lshl_add_u32 v22, v23, 3, v22\n"
    "v_add_u32 v240, s53, v22\n"
    "v_xor_b32 v241, 32, v240\n"
    "v_xor_b32 v242, 64, v240\n"
    "v_xor_b32 v243, 96, v240\n"
    "v_lshlrev_b32 v25, 7, v255\n"
    "s_lshl_b32 s84, s59, 2\n"
    "v_add_u32 v246, s84, v249\n"
    "s_lshr_b32 s89, s67, 1\n"
    "s_lshl_b32 s89, s89, 11\n"
    "s_add_u32 s89, s89, 32896\n"
    "s_waitcnt vmcnt(0)\n"
    "s_mov_b64 exec, 1\n"
    "ds_add_rtn_u32 v20, v249, v248 offset:4\n"
    "s_waitcnt lgkmcnt(0)\n"
    "v_readfirstlane_b32 s91, v20\n"
    "s_cmp_eq_u32 s91, 3\n"
    "s_cbranch_scc0 .Lgm_arr_14\n"
    "buffer_store_dwordx2 v[250:251], off, s[24:27], s73 sc1\n"
    "s_waitcnt vmcnt(0)\n"
    ".Lgm_arr_14:\n"
    "s_mov_b64 exec, -1\n"
    "s_mov_b32 s90, 1048576\n"
    ".Lgm_lw_15:\n"
    "ds_read_b32 v20, v249 offset:12\n"
    "s_waitcnt lgkmcnt(0)\n"
    "v_readfirstlane_b32 s91, v20\n"
    "s_cmp_ge_u32 s91, 8\n"
    "s_cbranch_scc1 .Lgm_lwd_16\n"
    "s_sub_u32 s90, s90, 1\n"
    "s_cbranch_scc0 .Lgm_lw_15\n"
    ".Lgm_lwd_16:\n"
    "s_setprio 1\n"
    "v_add_u32 v250, 7168, v253\n"
    "s_mul_i32 s88, s49, 7\n"
    "s_add_u32 m0, s56, s49\n"
    "s_add_u32 s88, s88, s52\n"
    "buffer_load_dwordx4 v253, s[32:35], s88 offen lds\n"
    "s_add_u32 s49, s49, 1024\n"
    "s_mul_i32 s88, s49, 7\n"
    "s_add_u32 m0, s56, s49\n"
    "s_add_u32 s88, s88, s52\n"
    "buffer_load_dwordx4 v253, s[32:35], s88 offen lds\n"
    "s_add_u32 s49, s49, 1024\n"
    "s_cmp_eq_u32 s49, 14336\n"
    "s_cselect_b32 s49, 0, s49\n"
    "s_waitcnt vmcnt(4)\n"
    "s_mul_i32 s88, s49, 7\n"
    "s_add_u32 m0, s56, s49\n"
    "s_add_u32 s88, s88, s52\n"
    "buffer_load_dwordx4 v253, s[32:35], s88 offen lds\n"
    "s_add_u32 s49, s49, 1024\n"
    "s_mul_i32 s88, s49, 7\n"
    "s_add_u32 m0, s56, s49\n"
    "s_add_u32 s88, s88, s52\n"
    "buffer_load_dwordx4 v253, s[32:35], s88 offen lds\n"
    "s_add_u32 s49, s49, 1024\n"
    "s_cmp_eq_u32 s49, 14336\n"
    "s_cselect_b32 s49, 0, s49\n"
    "s_waitcnt vmcnt(4)\n"
    "s_mul_i32 s88, s49, 7\n"
    "s_add_u32 m0, s56, s49\n"
    "s_add_u32 s88, s88, s52\n"
    "buffer_load_dwordx4 v253, s[32:35], s88 offen lds\n"
    "s_add_u32 s49, s49, 1024\n"
    "s_mul_i32 s88, s49, 7\n"
    "s_add_u32 m0, s56, s49\n"
    "s_add_u32 s88, s88, s52\n"
    "buffer_load_dwordx4 v253, s[32:35], s88 offen lds\n"
    "s_add_u32 s49, s49, 1024\n"
    "s_cmp_eq_u32 s49, 14336\n"
    "s_cselect_b32 s49, 0, s49\n"
    "s_waitcnt vmcnt(4)\n"
    "s_mul_i32 s88, s49, 7\n"
    "s_add_u32 m0, s56, s49\n"
    "s_add_u32 s88, s88, s52\n"
    "buffer_load_dwordx4 v253, s[32:35], s88 offen lds\n"
    "s_add_u32 s49, s49, 1024\n"
    "s_mul_i32 s88, s49, 7\n"
    "s_add_u32 m0, s56, s49\n"
    "s_add_u32 s88, s88, s52\n"
    "buffer_load_dwordx4 v253, s[32:35], s88 offen lds\n"
    "s_add_u32 s49, s49, 1024\n"
    "s_cmp_eq_u32 s49, 14336\n"
    "s_cselect_b32 s49, 0, s49\n"
    "s_waitcnt vmcnt(4)\n"
    "s_mul_i32 s88, s49, 7\n"
    "s_add_u32 m0, s56, s49\n"
    "s_add_u32 s88, s88, s52\n"
    "buffer_load_dwordx4 v253, s[32:35], s88 offen lds\n"
    "s_add_u32 s49, s49, 1024\n"
    "s_mul_i32 s88, s49, 7\n"
    "s_add_u32 m0, s56, s49\n"
    "s_add_u32 s88, s88, s52\n"
    "buffer_load_dwordx4 v253, s[32:35], s88 offen lds\n"
    "s_add_u32 s49, s49, 1024\n"
    "s_cmp_eq_u32 s49, 14336\n"
    "s_cselect_b32 s49, 0, s49\n"
    "s_waitcnt vmcnt(4)\n"
    "s_mul_i32 s88, s49, 7\n"
    "s_add_u32 m0, s56, s49\n"
    "s_add_u32 s88, s88, s52\n"
    "buffer_load_dwordx4 v253, s[32:35], s88 offen lds\n"
    "s_add_u32 s49, s49, 1024\n"
    "s_mul_i32 s88, s49, 7\n"
    "s_add_u32 m0, s56, s49\n"
    "s_add_u32 s88, s88, s52\n"
    "buffer_load_dwordx4 v253, s[32:35], s88 offen lds\n"
    "s_add_u32 s49, s49, 1024\n"
    "s_cmp_eq_u32 s49, 14336\n"
    "s_cselect_b32 s49, 0, s49\n"
    "s_waitcnt vmcnt(4)\n"
    "s_mul_i32 s88, s49, 7\n"
    "s_add_u32 m0, s56, s49\n"
    "s_add_u32 s88, s88, s52\n"
    "buffer_load_dwordx4 v253, s[32:35], s88 offen lds\n"
    "s_add_u32 s49, s49, 1024\n"
    "s_mul_i32 s88, s49, 7\n"
    "s_add_u32 m0, s56, s49\n"
    "s_add_u32 s88, s88, s52\n"
    "buffer_load_dwordx4 v253, s[32:35], s88 offen lds\n"
    "s_add_u32 s49, s49, 1024\n"
    "s_cmp_eq_u32 s49, 14336\n"
    "s_cselect_b32 s49, 0, s49\n"
    "s_waitcnt vmcnt(4)\n"
    "s_add_u32 m0, s53, 12288\n"
    "s_add_u32 s58, s58, 14336\n"
    "buffer_load_dwordx4 v253, s[28:31], s58 offen lds\n"
    "s_add_u32 m0, s53, 13312\n"
    "s_nop 0\n"
    "buffer_load_dwordx4 v250, s[28:31], s58 offen lds\n"
    "s_waitcnt vmcnt(4)\n"
    "s_add_u32 m0, s53, 14336\n"
    "s_add_u32 s58, s58, 14336\n"
    "buffer_load_dwordx4 v253, s[28:31], s58 offen lds\n"
    "s_add_u32 m0, s53, 15360\n"
    "s_nop 0\n"
    "buffer_load_dwordx4 v250, s[28:31], s58 offen lds\n"
    "s_cmp_eq_u32 s58, s98\n"
    "s_cselect_b32 s58, s94, s58\n"
    "s_waitcnt vmcnt(4)\n"
    "s_add_u32 m0, s53, 16384\n"
    "s_add_u32 s58, s58, 14336\n"
    "buffer_load_dwordx4 v253, s[28:31], s58 offen lds\n"
    "s_add_u32 m0, s53, 17408\n"
    "s_nop 0\n"
    "buffer_load_dwordx4 v250, s[28:31], s58 offen lds\n"
    "s_waitcnt vmcnt(4)\n"
    "s_add_u32 m0, s53, 18432\n"
    "s_add_u32 s58, s58, 14336\n"
    "buffer_load_dwordx4 v253, s[28:31], s58 offen lds\n"
    "s_add_u32 m0, s53, 19456\n"
    "s_nop 0\n"
    "buffer_load_dwordx4 v250, s[28:31], s58 offen lds\n"
    "s_cmp_eq_u32 s58, s98\n"
    "s_cselect_b32 s58, s94, s58\n"
    "s_waitcnt vmcnt(4)\n"
    "s_add_u32 m0, s53, 20480\n"
    "s_add_u32 s58, s58, 14336\n"
    "buffer_load_dwordx4 v253, s[28:31], s58 offen lds\n"
    "s_add_u32 m0, s53, 21504\n"
    "s_nop 0\n"
    "buffer_load_dwordx4 v250, s[28:31], s58 offen lds\n"
    "s_waitcnt vmcnt(4)\n"
    "s_waitcnt vmcnt(8)\n"
    "ds_read_b64_tr_b16 v[64:65], v240 offset:12288\n"
    "ds_read_b64_tr_b16 v[68:69], v241 offset:12288\n"
    "ds_read_b64_tr_b16 v[72:73], v242 offset:12288\n"
    "ds_read_b64_tr_b16 v[76:77], v243 offset:12288\n"
    "s_add_u32 m0, s53, 22528\n"
    "s_add_u32 s58, s58, 14336\n"
    "buffer_load_dwordx4 v253, s[28:31], s58 offen lds\n"
    "s_add_u32 m0, s53, 23552\n"
    "s_nop 0\n"
    "buffer_load_dwordx4 v250, s[28:31], s58 offen lds\n"
    "s_cmp_eq_u32 s58, s98\n"
    "s_cselect_b32 s58, s94, s58\n"
    "s_waitcnt vmcnt(4)\n"
    "s_waitcnt vmcnt(8)\n"
    "ds_read_b64_tr_b16 v[66:67], v240 offset:14336\n"
    "ds_read_b64_tr_b16 v[70:71], v241 offset:14336\n"
    "ds_read_b64_tr_b16 v[74:75], v242 offset:14336\n"
    "ds_read_b64_tr_b16 v[78:79], v243 offset:14336\n"
    "s_waitcnt lgkmcnt(4)\n"
    "s_add_u32 m0, s53, 12288\n"
    "s_add_u32 s58, s58, 14336\n"
    "buffer_load_dwordx4 v253, s[28:31], s58 offen lds\n"
    "s_add_u32 m0, s53, 13312\n"
    "s_nop 0\n"
    "buffer_load_dwordx4 v250, s[28:31], s58 offen lds\n"
    "s_waitcnt vmcnt(4)\n"
    "s_waitcnt vmcnt(8)\n"
    "ds_read_b64_tr_b16 v[80:81], v240 offset:16384\n"
    "ds_read_b64_tr_b16 v[84:85], v241 offset:16384\n"
    "ds_read_b64_tr_b16 v[88:89], v242 offset:16384\n"
    "ds_read_b64_tr_b16 v[92:93], v243 offset:16384\n"
    "s_waitcnt lgkmcnt(4)\n"
    "s_add_u32 m0, s53, 14336\n"
    "s_add_u32 s58, s58, 14336\n"
    "buffer_load_dwordx4 v253, s[28:31], s58 offen lds\n"
    "s_add_u32 m0, s53, 15360\n"
    "s_nop 0\n"
    "buffer_load_dwordx4 v250, s[28:31], s58 offen lds\n"
    "s_cmp_eq_u32 s58, s98\n"
    "s_cselect_b32 s58, s94, s58\n"
    "s_waitcnt vmcnt(4)\n"
    "s_waitcnt vmcnt(8)\n"
    "ds_read_b64_tr_b16 v[82:83], v240 offset:18432\n"
    "ds_read_b64_tr_b16 v[86:87], v241 offset:18432\n"
    "ds_read_b64_tr_b16 v[90:91], v242 offset:18432\n"
    "ds_read_b64_tr_b16 v[94:95], v243 offset:18432\n"
    "s_waitcnt lgkmcnt(4)\n"
    "s_add_u32 m0, s53, 16384\n"
    "s_add_u32 s58, s58, 14336\n"
    "buffer_load_dwordx4 v253, s[28:31], s58 offen lds\n"
    "s_add_u32 m0, s53, 17408\n"
    "s_nop 0\n"
    "buffer_load_dwordx4 v250, s[28:31], s58 offen lds\n"
    "s_waitcnt vmcnt(4)\n"
    "s_waitcnt vmcnt(8)\n"
    "ds_read_b64_tr_b16 v[96:97], v240 offset:20480\n"
    "ds_read_b64_tr_b16 v[100:101], v241 offset:20480\n"
    "ds_read_b64_tr_b16 v[104:105], v242 offset:20480\n"
    "ds_read_b64_tr_b16 v[108:109], v243 offset:20480\n"
    "s_waitcnt lgkmcnt(4)\n"
    "s_add_u32 m0, s53, 18432\n"
    "s_add_u32 s58, s58, 14336\n"
    "buffer_load_dwordx4 v253, s[28:31], s58 offen lds\n"
    "s_add_u32 m0, s53, 19456\n"
    "s_nop 0\n"
    "buffer_load_dwordx4 v250, s[28:31], s58 offen lds\n"
    "s_cmp_eq_u32 s58, s98\n"
    "s_cselect_b32 s58, s94, s58\n"
    "s_waitcnt vmcnt(4)\n"
    "s_waitcnt vmcnt(8)\n"
    "ds_read_b64_tr_b16 v[98:99], v240 offset:22528\n"
    "ds_read_b64_tr_b16 v[102:103], v241 offset:22528\n"
    "ds_read_b64_tr_b16 v[106:107], v242 offset:22528\n"
    "ds_read_b64_tr_b16 v[110:111], v243 offset:22528\n"
    "s_waitcnt lgkmcnt(4)\n"
    "s_add_u32 m0, s53, 20480\n"
    "s_add_u32 s58, s58, 14336\n"
    "buffer_load_dwordx4 v253, s[28:31], s58 offen lds\n"
    "s_add_u32 m0, s53, 21504\n"
    "s_nop 0\n"
    "buffer_load_dwordx4 v250, s[28:31], s58 offen lds\n"
    "s_waitcnt vmcnt(4)\n"
    "s_waitcnt vmcnt(8)\n"
    "ds_read_b64_tr_b16 v[112:113], v240 offset:12288\n"
    "ds_read_b64_tr_b16 v[116:117], v241 offset:12288\n"
    "ds_read_b64_tr_b16 v[120:121], v242 offset:12288\n"
    "ds_read_b64_tr_b16 v[124:125], v243 offset:12288\n"
    "s_waitcnt lgkmcnt(4)\n"
    "s_add_u32 m0, s53, 22528\n"
    "s_add_u32 s58, s58, 14336\n"
    "buffer_load_dwordx4 v253, s[28:31], s58 offen lds\n"
    "s_add_u32 m0, s53, 23552\n"
    "s_nop 0\n"
    "buffer_load_dwordx4 v250, s[28:31], s58 offen lds\n"
    "s_cmp_eq_u32 s58, s98\n"
    "s_cselect_b32 s58, s94, s58\n"
    "s_waitcnt vmcnt(4)\n"
    "s_waitcnt vmcnt(8)\n"
    "ds_read_b64_tr_b16 v[114:115], v240 offset:14336\n"
    "ds_read_b64_tr_b16 v[118:119], v241 offset:14336\n"
    "ds_read_b64_tr_b16 v[122:123], v242 offset:14336\n"
    "ds_read_b64_tr_b16 v[126:127], v243 offset:14336\n"
    "s_waitcnt lgkmcnt(4)\n"
    "s_bfm_b64 exec, 16, 0\n"
    "buffer_load_dwordx2 v[26:27], v25, s[24:27], s89 offen sc1\n"
    "s_mov_b64 exec, -1\n"
    "s_add_u32 m0, s53, 12288\n"
    "s_add_u32 s58, s58, 14336\n"
    "buffer_load_dwordx4 v253, s[28:31], s58 offen lds\n"
    "s_add_u32 m0, s53, 13312\n"
    "s_nop 0\n"
    "buffer_load_dwordx4 v250, s[28:31], s58 offen lds\n"
    "s_waitcnt vmcnt(4)\n"
    "s_waitcnt vmcnt(9)\n"
    "ds_read_b64_tr_b16 v[128:129], v240 offset:16384\n"
    "ds_read_b64_tr_b16 v[132:133], v241 offset:16384\n"
    "ds_read_b64_tr_b16 v[136:137], v242 offset:16384\n"
    "ds_read_b64_tr_b16 v[140:141], v243 offset:16384\n"
    "s_waitcnt lgkmcnt(4)\n"
    "s_add_u32 m0, s53, 14336\n"
    "s_add_u32 s58, s58, 14336\n"
    "buffer_load_dwordx4 v253, s[28:31], s58 offen lds\n"
    "s_add_u32 m0, s53, 15360\n"
    "s_nop 0\n"
    "buffer_load_dwordx4 v250, s[28:31], s58 offen lds\n"
    "s_cmp_eq_u32 s58, s98\n"
    "s_cselect_b32 s58, s94, s58\n"
    "s_waitcnt vmcnt(4)\n"
    "s_waitcnt vmcnt(9)\n"
    "ds_read_b64_tr_b16 v[130:131], v240 offset:18432\n"
    "ds_read_b64_tr_b16 v[134:135], v241 offset:18432\n"
    "ds_read_b64_tr_b16 v[138:139], v242 offset:18432\n"
    "ds_read_b64_tr_b16 v[142:143], v243 offset:18432\n"
    "s_waitcnt lgkmcnt(4)\n"
    "s_add_u32 m0, s53, 16384\n"
    "s_add_u32 s58, s58, 14336\n"
    "buffer_load_dwordx4 v253, s[28:31], s58 offen lds\n"
    "s_add_u32 m0, s53, 17408\n"
    "s_nop 0\n"
    "buffer_load_dwordx4 v250, s[28:31], s58 offen lds\n"
    "s_waitcnt vmcnt(6)\n"
    "s_bfm_b64 exec, 16, 0\n"
    "v_cmp_ne_u64 vcc, s[74:75], v[26:27]\n"
    "s_and_b64 vcc, exec, vcc\n"
    "s_cbranch_scc0 .Lgm_pd_17\n"
    "s_bfm_b64 exec, 16, 0\n"
    "s_mov_b32 s91, 65536\n"
    ".Lgm_poll_18:\n"
    "buffer_load_dwordx2 v[26:27], v25, s[24:27], s89 offen sc1\n"
    "s_waitcnt vmcnt(0)\n"
    "v_cmp_ne_u64 vcc, s[74:75], v[26:27]\n"
    "s_and_b64 vcc, exec, vcc\n"
    "s_cbranch_scc0 .Lgm_polld_19\n"
    "s_sleep 4\n"
    "s_sub_u32 s91, s91, 1\n"
    "s_cbranch_scc0 .Lgm_poll_18\n"
    ".Lgm_polld_19:\n"
    "s_mov_b64 exec, -1\n"
    ".Lgm_pd_17:\n"
    "s_mov_b64 exec, -1\n"
    "s_lshl_b32 s84, s67, 2\n"
    "s_add_u32 s84, s84, s59\n"
    "s_lshl_b32 s84, s84, 6\n"
    "s_add_u32 s84, s84, 98560\n"
    "v_and_b32 v20, 15, v255\n"
    "v_lshlrev_b32 v20, 2, v20\n"
    "buffer_load_dword v21, v20, s[24:27], s84 offen sc1\n"
    "s_lshl_b32 s84, s59, 8\n"
    "s_add_u32 s84, s84, 156672\n"
    "v_lshl_add_u32 v20, v255, 2, s84\n"
    "buffer_load_dwordx4 v[160:163], v244, s[24:27], s72 offen offset:0 sc1\n"
    "buffer_load_dwordx4 v[164:167], v244, s[24:27], s72 offen offset:256 sc1\n"
    "s_waitcnt vmcnt(4)\n"
    "buffer_load_dwordx4 v[168:171], v245, s[24:27], s72 offen offset:0 sc1\n"
    "buffer_load_dwordx4 v[172:175], v245, s[24:27], s72 offen offset:256 sc1\n"
    "s_add_u32 s72, s72, 2048\n"
    "s_cmp_eq_u32 s72, s99\n"
    "s_cselect_b32 s72, s95, s72\n"
    "s_waitcnt vmcnt(4)\n"
    "buffer_load_dwordx4 v[176:179], v244, s[24:27], s72 offen offset:0 sc1\n"
    "buffer_load_dwordx4 v[180:183], v244, s[24:27], s72 offen offset:256 sc1\n"
    "s_waitcnt vmcnt(4)\n"
    "buffer_load_dwordx4 v[184:187], v245, s[24:27], s72 offen offset:0 sc1\n"
    "buffer_load_dwordx4 v[188:191], v245, s[24:27], s72 offen offset:256 sc1\n"
    "s_add_u32 s72, s72, 2048\n"
    "s_cmp_eq_u32 s72, s99\n"
    "s_cselect_b32 s72, s95, s72\n"
    "s_waitcnt vmcnt(4)\n"
    "buffer_load_dwordx4 v[192:195], v244, s[24:27], s72 offen offset:0 sc1\n"
    "buffer_load_dwordx4 v[196:199], v244, s[24:27], s72 offen offset:256 sc1\n"
    "s_waitcnt vmcnt(4)\n"
    "buffer_load_dwordx4 v[200:203], v245, s[24:27], s72 offen offset:0 sc1\n"
    "buffer_load_dwordx4 v[204:207], v245, s[24:27], s72 offen offset:256 sc1\n"
    "s_add_u32 s72, s72, 2048\n"
    "s_cmp_eq_u32 s72, s99\n"
    "s_cselect_b32 s72, s95, s72\n"
    "s_waitcnt vmcnt(4)\n"
    "buffer_load_dwordx4 v[208:211], v244, s[24:27], s72 offen offset:0 sc1\n"
    "buffer_load_dwordx4 v[212:215], v244, s[24:27], s72 offen offset:256 sc1\n"
    "s_waitcnt vmcnt(4)\n"
    "buffer_load_dwordx4 v[216:219], v245, s[24:27], s72 offen offset:0 sc1\n"
    "buffer_load_dwordx4 v[220:223], v245, s[24:27], s72 offen offset:256 sc1\n"
    "s_add_u32 s72, s72, 2048\n"
    "s_cmp_eq_u32 s72, s99\n"
    "s_cselect_b32 s72, s95, s72\n"
    "s_waitcnt vmcnt(4)\n"
    "buffer_load_dwordx4 v[224:227], v244, s[24:27], s72 offen offset:0 sc1\n"
    "buffer_load_dwordx4 v[228:231], v244, s[24:27], s72 offen offset:256 sc1\n"
    "s_waitcnt vmcnt(4)\n"
    "buffer_load_dwordx4 v[232:235], v245, s[24:27], s72 offen offset:0 sc1\n"
    "buffer_load_dwordx4 v[236:239], v245, s[24:27], s72 offen offset:256 sc1\n"
    "s_add_u32 s72, s72, 2048\n"
    "s_cmp_eq_u32 s72, s99\n"
    "s_cselect_b32 s72, s95, s72\n"
    "s_waitcnt vmcnt(4)\n"
    "s_waitcnt vmcnt(18)\n"
    "ds_write_b32 v20, v21\n"
    "s_mov_b64 exec, 1\n"
    "ds_add_u32 v249, v248 offset:20\n"
    "s_mov_b64 exec, -1\n"
    "v_mfma_f32_16x16x32_bf16 v[0:3], v[64:67], v[160:163], 0\n"
    "ds_read_b64_tr_b16 v[144:145], v240 offset:20480\n"
    "v_mfma_f32_16x16x32_bf16 v[4:7], v[68:71], v[160:163], 0\n"
    "ds_read_b64_tr_b16 v[148:149], v241 offset:20480\n"
    "v_mfma_f32_16x16x32_bf16 v[8:11], v[72:75], v[160:163], 0\n"
    "ds_read_b64_tr_b16 v[152:153], v242 offset:20480\n"
    "v_mfma_f32_16x16x32_bf16 v[12:15], v[76:79], v[160:163], 0\n"
    "ds_read_b64_tr_b16 v[156:157], v243 offset:20480\n"
    "v_mfma_f32_16x16x32_bf16 v[16:19], v[64:67], v[164:167], 0\n"
    "s_waitcnt lgkmcnt(6)\n"
    "s_add_u32 m0, s53, 18432\n"
    "s_add_u32 s58, s58, 14336\n"
    "v_mfma_f32_16x16x32_bf16 v[20:23], v[68:71], v[164:167], 0\n"
    "buffer_load_dwordx4 v253, s[28:31], s58 offen lds\n"
    "s_add_u32 m0, s53, 19456\n"
    "v_mfma_f32_16x16x32_bf16 v[24:27], v[72:75], v[164:167], 0\n"
    "buffer_load_dwordx4 v250, s[28:31], s58 offen lds\n"
    "s_cmp_eq_u32 s58, s98\n"
    "s_cselect_b32 s58, s94, s58\n"
    "v_mfma_f32_16x16x32_bf16 v[28:31], v[76:79], v[164:167], 0\n"
    "s_waitcnt vmcnt(4)\n"
    "buffer_load_dwordx4 v[160:163], v244, s[24:27], s72 offen offset:0 sc1\n"
    "buffer_load_dwordx4 v[164:167], v244, s[24:27], s72 offen offset:256 sc1\n"
    "s_waitcnt vmcnt(4)\n"
    "s_waitcnt vmcnt(20)\n"
    "v_mfma_f32_16x16x32_bf16 v[32:35], v[64:67], v[168:171], 0\n"
    "ds_read_b64_tr_b16 v[146:147], v240 offset:22528\n"
    "v_mfma_f32_16x16x32_bf16 v[36:39], v[68:71], v[168:171], 0\n"
    "ds_read_b64_tr_b16 v[150:151], v241 offset:22528\n"
    "v_mfma_f32_16x16x32_bf16 v[40:43], v[72:75], v[168:171], 0\n"
    "ds_read_b64_tr_b16 v[154:155], v242 offset:22528\n"
    "v_mfma_f32_16x16x32_bf16 v[44:47], v[76:79], v[168:171], 0\n"
    "ds_read_b64_tr_b16 v[158:159], v243 offset:22528\n"
    "v_mfma_f32_16x16x32_bf16 v[48:51], v[64:67], v[172:175], 0\n"
    "s_waitcnt lgkmcnt(4)\n"
    "s_add_u32 m0, s53, 20480\n"
    "s_add_u32 s58, s58, 14336\n"
    "v_mfma_f32_16x16x32_bf16 v[52:55], v[68:71], v[172:175], 0\n"
    "buffer_load_dwordx4 v253, s[28:31], s58 offen lds\n"
    "s_add_u32 m0, s53, 21504\n"
    "v_mfma_f32_16x16x32_bf16 v[56:59], v[72:75], v[172:175], 0\n"
    "buffer_load_dwordx4 v250, s[28:31], s58 offen lds\n"
    "v_mfma_f32_16x16x32_bf16 v[60:63], v[76:79], v[172:175], 0\n"
    "s_waitcnt vmcnt(4)\n"
    "buffer_load_dwordx4 v[168:171], v245, s[24:27], s72 offen offset:0 sc1\n"
    "buffer_load_dwordx4 v[172:175], v245, s[24:27], s72 offen offset:256 sc1\n"
    "s_add_u32 s72, s72, 2048\n"
    "s_cmp_eq_u32 s72, s99\n"
    "s_cselect_b32 s72, s95, s72\n"
    "s_waitcnt vmcnt(4)\n"
    "s_waitcnt vmcnt(22)\n"
    "v_mfma_f32_16x16x32_bf16 v[0:3], v[80:83], v[176:179], v[0:3]\n"
    "ds_read_b64_tr_b16 v[64:65], v240 offset:12288\n"
    "v_mfma_f32_16x16x32_bf16 v[4:7], v[84:87], v[176:179], v[4:7]\n"
    "ds_read_b64_tr_b16 v[68:69], v241 offset:12288\n"
    "v_mfma_f32_16x16x32_bf16 v[8:11], v[88:91], v[176:179], v[8:11]\n"
    "ds_read_b64_tr_b16 v[72:73], v242 offset:12288\n"
    "v_mfma_f32_16x16x32_bf16 v[12:15], v[92:95], v[176:179], v[12:15]\n"
    "ds_read_b64_tr_b16 v[76:77], v243 offset:12288\n"
    "v_mfma_f32_16x16x32_bf16 v[16:19], v[80:83], v[180:183], v[16:19]\n"
    "s_waitcnt lgkmcnt(4)\n"
    "s_add_u32 m0, s53, 22528\n"
    "s_add_u32 s58, s58, 14336\n"
    "v_mfma_f32_16x16x32_bf16 v[20:23], v[84:87], v[180:183], v[20:23]\n"
    "buffer_load_dwordx4 v253, s[28:31], s58 offen lds\n"
    "s_add_u32 m0, s53, 23552\n"
    "v_mfma_f32_16x16x32_bf16 v[24:27], v[88:91], v[180:183], v[24:27]\n"
    "buffer_load_dwordx4 v250, s[28:31], s58 offen lds\n"
    "s_cmp_eq_u32 s58, s98\n"
    "s_cselect_b32 s58, s94, s58\n"
    "v_mfma_f32_16x16x32_bf16 v[28:31], v[92:95], v[180:183], v[28:31]\n"
    "s_waitcnt vmcnt(4)\n"
    "buffer_load_dwordx4 v[176:179], v244, s[24:27], s72 offen offset:0 sc1\n"
    "buffer_load_dwordx4 v[180:183], v244, s[24:27], s72 offen offset:256 sc1\n"
    "s_waitcnt vmcnt(4)\n"
    "s_waitcnt vmcnt(24)\n"
    "v_mfma_f32_16x16x32_bf16 v[32:35], v[80:83], v[184:187], v[32:35]\n"
    "ds_read_b64_tr_b16 v[66:67], v240 offset:14336\n"
    "v_mfma_f32_16x16x32_bf16 v[36:39], v[84:87], v[184:187], v[36:39]\n"
    "ds_read_b64_tr_b16 v[70:71], v241 offset:14336\n"
    "v_mfma_f32_16x16x32_bf16 v[40:43], v[88:91], v[184:187], v[40:43]\n"
    "ds_read_b64_tr_b16 v[74:75], v242 offset:14336\n"
    "v_mfma_f32_16x16x32_bf16 v[44:47], v[92:95], v[184:187], v[44:47]\n"
    "ds_read_b64_tr_b16 v[78:79], v243 offset:14336\n"
    "v_mfma_f32_16x16x32_bf16 v[48:51], v[80:83], v[188:191], v[48:51]\n"
    "s_waitcnt lgkmcnt(4)\n"
    "s_add_u32 m0, s53, 12288\n"
    "s_add_u32 s58, s58, 14336\n"
    "v_mfma_f32_16x16x32_bf16 v[52:55], v[84:87], v[188:191], v[52:55]\n"
    "buffer_load_dwordx4 v253, s[28:31], s58 offen lds\n"
    "s_add_u32 m0, s53, 13312\n"
    "v_mfma_f32_16x16x32_bf16 v[56:59], v[88:91], v[188:191], v[56:59]\n"
    "buffer_load_dwordx4 v250, s[28:31], s58 offen lds\n"
    "v_mfma_f32_16x16x32_bf16 v[60:63], v[92:95], v[188:191], v[60:63]\n"
    "s_waitcnt vmcnt(4)\n"
    "buffer_load_dwordx4 v[184:187], v245, s[24:27], s72 offen offset:0 sc1\n"
    "buffer_load_dwordx4 v[188:191], v245, s[24:27], s72 offen offset:256 sc1\n"
    "s_add_u32 s72, s72, 2048\n"
    "s_cmp_eq_u32 s72, s99\n"
    "s_cselect_b32 s72, s95, s72\n"
    "s_waitcnt vmcnt(4)\n"
    "s_waitcnt vmcnt(26)\n"
    "v_mfma_f32_16x16x32_bf16 v[0:3], v[96:99], v[192:195], v[0:3]\n"
    "ds_read_b64_tr_b16 v[80:81], v240 offset:16384\n"
    "v_mfma_f32_16x16x32_bf16 v[4:7], v[100:103], v[192:195], v[4:7]\n"
    "ds_read_b64_tr_b16 v[84:85], v241 offset:16384\n"
    "v_mfma_f32_16x16x32_bf16 v[8:11], v[104:107], v[192:195], v[8:11]\n"
    "ds_read_b64_tr_b16 v[88:89], v242 offset:16384\n"
    "v_mfma_f32_16x16x32_bf16 v[12:15], v[108:111], v[192:195], v[12:15]\n"
    "ds_read_b64_tr_b16 v[92:93], v243 offset:16384\n"
    "v_mfma_f32_16x16x32_bf16 v[16:19], v[96:99], v[196:199], v[16:19]\n"
    "s_waitcnt lgkmcnt(4)\n"
    "s_add_u32 m0, s53, 14336\n"
    "s_add_u32 s58, s58, 14336\n"
    "v_mfma_f32_16x16x32_bf16 v[20:23], v[100:103], v[196:199], v[20:23]\n"
    "buffer_load_dwordx4 v253, s[28:31], s58 offen lds\n"
    "s_add_u32 m0, s53, 15360\n"
    "v_mfma_f32_16x16x32_bf16 v[24:27], v[104:107], v[196:199], v[24:27]\n"
    "buffer_load_dwordx4 v250, s[28:31], s58 offen lds\n"
    "s_cmp_eq_u32 s58, s98\n"
    "s_cselect_b32 s58, s94, s58\n"
    "v_mfma_f32_16x16x32_bf16 v[28:31], v[108:111], v[196:199], v[28:31]\n"
    "s_waitcnt vmcnt(4)\n"
    "buffer_load_dwordx4 v[192:195], v244, s[24:27], s72 offen offset:0 sc1\n"
    "buffer_load_dwordx4 v[196:199], v244, s[24:27], s72 offen offset:256 sc1\n"
    "s_waitcnt vmcnt(4)\n"
    "s_waitcnt vmcnt(18)\n"
    "v_mfma_f32_16x16x32_bf16 v[32:35], v[96:99], v[200:203], v[32:35]\n"
    "ds_read_b64_tr_b16 v[82:83], v240 offset:18432\n"
    "v_mfma_f32_16x16x32_bf16 v[36:39], v[100:103], v[200:203], v[36:39]\n"
    "ds_read_b64_tr_b16 v[86:87], v241 offset:18432\n"
    "v_mfma_f32_16x16x32_bf16 v[40:43], v[104:107], v[200:203], v[40:43]\n"
    "ds_read_b64_tr_b16 v[90:91], v242 offset:18432\n"
    "v_mfma_f32_16x16x32_bf16 v[44:47], v[108:111], v[200:203], v[44:47]\n"
    "ds_read_b64_tr_b16 v[94:95], v243 offset:18432\n"
    "v_mfma_f32_16x16x32_bf16 v[48:51], v[96:99], v[204:207], v[48:51]\n"
    "s_waitcnt lgkmcnt(4)\n"
    "s_add_u32 m0, s53, 16384\n"
    "s_add_u32 s58, s58, 14336\n"
    "v_mfma_f32_16x16x32_bf16 v[52:55], v[100:103], v[204:207], v[52:55]\n"
    "buffer_load_dwordx4 v253, s[28:31], s58 offen lds\n"
    "s_add_u32 m0, s53, 17408\n"
    "v_mfma_f32_16x16x32_bf16 v[56:59], v[104:107], v[204:207], v[56:59]\n"
    "buffer_load_dwordx4 v250, s[28:31], s58 offen lds\n"
    "v_mfma_f32_16x16x32_bf16 v[60:63], v[108:111], v[204:207], v[60:63]\n"
    "s_waitcnt vmcnt(4)\n"
    "buffer_load_dwordx4 v[200:203], v245, s[24:27], s72 offen offset:0 sc1\n"
    "buffer_load_dwordx4 v[204:207], v245, s[24:27], s72 offen offset:256 sc1\n"
    "s_add_u32 s72, s72, 2048\n"
    "s_cmp_eq_u32 s72, s99\n"
    "s_cselect_b32 s72, s95, s72\n"
    "s_waitcnt vmcnt(4)\n"
    "s_waitcnt vmcnt(18)\n"
    "v_mfma_f32_16x16x32_bf16 v[0:3], v[112:115], v[208:211], v[0:3]\n"
    "ds_read_b64_tr_b16 v[96:97], v240 offset:20480\n"
    "v_mfma_f32_16x16x32_bf16 v[4:7], v[116:119], v[208:211], v[4:7]\n"
    "ds_read_b64_tr_b16 v[100:101], v241 offset:20480\n"
    "v_mfma_f32_16x16x32_bf16 v[8:11], v[120:123], v[208:211], v[8:11]\n"
    "ds_read_b64_tr_b16 v[104:105], v242 offset:20480\n"
    "v_mfma_f32_16x16x32_bf16 v[12:15], v[124:127], v[208:211], v[12:15]\n"
    "ds_read_b64_tr_b16 v[108:109], v243 offset:20480\n"
    "v_mfma_f32_16x16x32_bf16 v[16:19], v[112:115], v[212:215], v[16:19]\n"
    "s_waitcnt lgkmcnt(4)\n"
    "s_add_u32 m0, s53, 18432\n"
    "s_add_u32 s58, s58, 14336\n"
    "v_mfma_f32_16x16x32_bf16 v[20:23], v[116:119], v[212:215], v[20:23]\n"
    "buffer_load_dwordx4 v253, s[28:31], s58 offen lds\n"
    "s_add_u32 m0, s53, 19456\n"
    "v_mfma_f32_16x16x32_bf16 v[24:27], v[120:123], v[212:215], v[24:27]\n"
    "buffer_load_dwordx4 v250, s[28:31], s58 offen lds\n"
    "s_cmp_eq_u32 s58, s98\n"
    "s_cselect_b32 s58, s94, s58\n"
    "v_mfma_f32_16x16x32_bf16 v[28:31], v[124:127], v[212:215], v[28:31]\n"
    "s_waitcnt vmcnt(4)\n"
    "buffer_load_dwordx4 v[208:211], v244, s[24:27], s72 offen offset:0 sc1\n"
    "buffer_load_dwordx4 v[212:215], v244, s[24:27], s72 offen offset:256 sc1\n"
    "s_waitcnt vmcnt(4)\n"
    "s_waitcnt vmcnt(18)\n"
    "v_mfma_f32_16x16x32_bf16 v[32:35], v[112:115], v[216:219], v[32:35]\n"
    "ds_read_b64_tr_b16 v[98:99], v240 offset:22528\n"
    "v_mfma_f32_16x16x32_bf16 v[36:39], v[116:119], v[216:219], v[36:39]\n"
    "ds_read_b64_tr_b16 v[102:103], v241 offset:22528\n"
    "v_mfma_f32_16x16x32_bf16 v[40:43], v[120:123], v[216:219], v[40:43]\n"
    "ds_read_b64_tr_b16 v[106:107], v242 offset:22528\n"
    "v_mfma_f32_16x16x32_bf16 v[44:47], v[124:127], v[216:219], v[44:47]\n"
    "ds_read_b64_tr_b16 v[110:111], v243 offset:22528\n"
    "v_mfma_f32_16x16x32_bf16 v[48:51], v[112:115], v[220:223], v[48:51]\n"
    "s_waitcnt lgkmcnt(4)\n"
    "s_add_u32 m0, s53, 20480\n"
    "s_add_u32 s58, s58, 14336\n"
    "v_mfma_f32_16x16x32_bf16 v[52:55], v[116:119], v[220:223], v[52:55]\n"
    "buffer_load_dwordx4 v253, s[28:31], s58 offen lds\n"
    "s_add_u32 m0, s53, 21504\n"
    "v_mfma_f32_16x16x32_bf16 v[56:59], v[120:123], v[220:223], v[56:59]\n"
    "buffer_load_dwordx4 v250, s[28:31], s58 offen lds\n"
    "v_mfma_f32_16x16x32_bf16 v[60:63], v[124:127], v[220:223], v[60:63]\n"
    "s_waitcnt vmcnt(4)\n"
    "buffer_load_dwordx4 v[216:219], v245, s[24:27], s72 offen offset:0 sc1\n"
    "buffer_load_dwordx4 v[220:223], v245, s[24:27], s72 offen offset:256 sc1\n"
    "s_add_u32 s72, s72, 2048\n"
    "s_cmp_eq_u32 s72, s99\n"
    "s_cselect_b32 s72, s95, s72\n"
    "s_waitcnt vmcnt(4)\n"
    "s_setprio 0\n"
    "s_mov_b32 s90, 1048576\n"
    ".Lgm_lw_20:\n"
    "ds_read_b32 v247, v246 offset:48\n"
    "s_waitcnt lgkmcnt(0)\n"
    "v_readfirstlane_b32 s91, v247\n"
    "s_cmp_ge_u32 s91, 1\n"
    "s_cbranch_scc1 .Lgm_lwd_21\n"
    "s_sleep 1\n"
    "s_sub_u32 s90, s90, 1\n"
    "s_cbranch_scc0 .Lgm_lw_20\n"
    ".Lgm_lwd_21:\n"
    "s_waitcnt vmcnt(18)\n"
    "v_mfma_f32_16x16x32_bf16 v[0:3], v[128:131], v[224:227], v[0:3]\n"
    "ds_read_b64_tr_b16 v[112:113], v240 offset:12288\n"
    "v_mfma_f32_16x16x32_bf16 v[4:7], v[132:135], v[224:227], v[4:7]\n"
    "ds_read_b64_tr_b16 v[116:117], v241 offset:12288\n"
    "v_mfma_f32_16x16x32_bf16 v[8:11], v[136:139], v[224:227], v[8:11]\n"
    "ds_read_b64_tr_b16 v[120:121], v242 offset:12288\n"
    "v_mfma_f32_16x16x32_bf16 v[12:15], v[140:143], v[224:227], v[12:15]\n"
    "ds_read_b64_tr_b16 v[124:125], v243 offset:12288\n"
    "v_mfma_f32_16x16x32_bf16 v[16:19], v[128:131], v[228:231], v[16:19]\n"
    "s_add_u32 m0, s53, 22528\n"
    "s_add_u32 s58, s58, 14336\n"
    "v_mfma_f32_16x16x32_bf16 v[20:23], v[132:135], v[228:231], v[20:23]\n"
    "buffer_load_dwordx4 v253, s[28:31], s58 offen lds\n"
    "s_add_u32 m0, s53, 23552\n"
    "v_mfma_f32_16x16x32_bf16 v[24:27], v[136:139], v[228:231], v[24:27]\n"
    "buffer_load_dwordx4 v250, s[28:31], s58 offen lds\n"
    "s_cmp_eq_u32 s58, s98\n"
    "s_cselect_b32 s58, s94, s58\n"
    "v_mfma_f32_16x16x32_bf16 v[28:31], v[140:143], v[228:231], v[28:31]\n"
    "s_waitcnt vmcnt(16)\n"
    "v_mfma_f32_16x16x32_bf16 v[32:35], v[128:131], v[232:235], v[32:35]\n"
    "ds_read_b64_tr_b16 v[114:115], v240 offset:14336\n"
    "v_mfma_f32_16x16x32_bf16 v[36:39], v[132:135], v[232:235], v[36:39]\n"
    "ds_read_b64_tr_b16 v[118:119], v241 offset:14336\n"
    "v_mfma_f32_16x16x32_bf16 v[40:43], v[136:139], v[232:235], v[40:43]\n"
    "ds_read_b64_tr_b16 v[122:123], v242 offset:14336\n"
    "v_mfma_f32_16x16x32_bf16 v[44:47], v[140:143], v[232:235], v[44:47]\n"
    "ds_read_b64_tr_b16 v[126:127], v243 offset:14336\n"
    "v_mfma_f32_16x16x32_bf16 v[48:51], v[128:131], v[236:239], v[48:51]\n"
    "s_waitcnt lgkmcnt(4)\n"
    "s_add_u32 m0, s53, 12288\n"
    "s_add_u32 s58, s58, 14336\n"
    "v_mfma_f32_16x16x32_bf16 v[52:55], v[132:135], v[236:239], v[52:55]\n"
    "buffer_load_dwordx4 v253, s[28:31], s58 offen lds\n"
    "s_add_u32 m0, s53, 13312\n"
    "v_mfma_f32_16x16x32_bf16 v[56:59], v[136:139], v[236:239], v[56:59]\n"
    "buffer_load_dwordx4 v250, s[28:31], s58 offen lds\n"
    "v_mfma_f32_16x16x32_bf16 v[60:63], v[140:143], v[236:239], v[60:63]\n"
    "buffer_load_dwordx4 v[224:227], v244, s[24:27], s72 offen offset:0 sc1\n"
    "buffer_load_dwordx4 v[228:231], v244, s[24:27], s72 offen offset:256 sc1\n"
    "s_waitcnt vmcnt(16)\n"
    "v_mfma_f32_16x16x32_bf16 v[0:3], v[144:147], v[160:163], v[0:3]\n"
    "ds_read_b64_tr_b16 v[128:129], v240 offset:16384\n"
    "v_mfma_f32_16x16x32_bf16 v[4:7], v[148:151], v[160:163], v[4:7]\n"
    "ds_read_b64_tr_b16 v[132:133], v241 offset:16384\n"
    "v_mfma_f32_16x16x32_bf16 v[8:11], v[152:155], v[160:163], v[8:11]\n"
    "ds_read_b64_tr_b16 v[136:137], v242 offset:16384\n"
    "v_mfma_f32_16x16x32_bf16 v[12:15], v[156:159], v[160:163], v[12:15]\n"
    "ds_read_b64_tr_b16 v[140:141], v243 offset:16384\n"
    "v_mfma_f32_16x16x32_bf16 v[16:19], v[144:147], v[164:167], v[16:19]\n"
    "s_waitcnt lgkmcnt(4)\n"
    "s_add_u32 m0, s53, 14336\n"
    "s_add_u32 s58, s58, 14336\n"
    "v_mfma_f32_16x16x32_bf16 v[20:23], v[148:151], v[164:167], v[20:23]\n"
    "buffer_load_dwordx4 v253, s[28:31], s58 offen lds\n"
    "s_add_u32 m0, s53, 15360\n"
    "v_mfma_f32_16x16x32_bf16 v[24:27], v[152:155], v[164:167], v[24:27]\n"
    "buffer_load_dwordx4 v250, s[28:31], s58 offen lds\n"
    "s_cmp_eq_u32 s58, s98\n"
    "s_cselect_b32 s58, s94, s58\n"
    "v_mfma_f32_16x16x32_bf16 v[28:31], v[156:159], v[164:167], v[28:31]\n"
    "s_waitcnt vmcnt(14)\n"
    "v_mfma_f32_16x16x32_bf16 v[32:35], v[144:147], v[168:171], v[32:35]\n"
    "ds_read_b64_tr_b16 v[130:131], v240 offset:18432\n"
    "v_mfma_f32_16x16x32_bf16 v[36:39], v[148:151], v[168:171], v[36:39]\n"
    "ds_read_b64_tr_b16 v[134:135], v241 offset:18432\n"
    "v_mfma_f32_16x16x32_bf16 v[40:43], v[152:155], v[168:171], v[40:43]\n"
    "ds_read_b64_tr_b16 v[138:139], v242 offset:18432\n"
    "v_mfma_f32_16x16x32_bf16 v[44:47], v[156:159], v[168:171], v[44:47]\n"
    "ds_read_b64_tr_b16 v[142:143], v243 offset:18432\n"
    "v_mfma_f32_16x16x32_bf16 v[48:51], v[144:147], v[172:175], v[48:51]\n"
    "s_waitcnt lgkmcnt(4)\n"
    "s_add_u32 m0, s53, 16384\n"
    "s_add_u32 s58, s58, 14336\n"
    "v_mfma_f32_16x16x32_bf16 v[52:55], v[148:151], v[172:175], v[52:55]\n"
    "buffer_load_dwordx4 v253, s[28:31], s58 offen lds\n"
    "s_add_u32 m0, s53, 17408\n"
    "v_mfma_f32_16x16x32_bf16 v[56:59], v[152:155], v[172:175], v[56:59]\n"
    "buffer_load_dwordx4 v250, s[28:31], s58 offen lds\n"
    "v_mfma_f32_16x16x32_bf16 v[60:63], v[156:159], v[172:175], v[60:63]\n"
    "buffer_load_dwordx4 v[232:235], v245, s[24:27], s72 offen offset:0 sc1\n"
    "buffer_load_dwordx4 v[236:239], v245, s[24:27], s72 offen offset:256 sc1\n"
    "s_add_u32 s72, s72, 2048\n"
    "s_cmp_eq_u32 s72, s99\n"
    "s_cselect_b32 s72, s95, s72\n"
    "s_waitcnt vmcnt(14)\n"
    "v_mfma_f32_16x16x32_bf16 v[0:3], v[64:67], v[176:179], v[0:3]\n"
    "ds_read_b64_tr_b16 v[144:145], v240 offset:20480\n"
    "v_mfma_f32_16x16x32_bf16 v[4:7], v[68:71], v[176:179], v[4:7]\n"
    "ds_read_b64_tr_b16 v[148:149], v241 offset:20480\n"
    "v_mfma_f32_16x16x32_bf16 v[8:11], v[72:75], v[176:179], v[8:11]\n"
    "ds_read_b64_tr_b16 v[152:153], v242 offset:20480\n"
    "v_mfma_f32_16x16x32_bf16 v[12:15], v[76:79], v[176:179], v[12:15]\n"
    "ds_read_b64_tr_b16 v[156:157], v243 offset:20480\n"
    "v_mfma_f32_16x16x32_bf16 v[16:19], v[64:67], v[180:183], v[16:19]\n"
    "s_waitcnt lgkmcnt(4)\n"
    "s_add_u32 m0, s53, 18432\n"
    "s_add_u32 s58, s58, 14336\n"
    "v_mfma_f32_16x16x32_bf16 v[20:23], v[68:71], v[180:183], v[20:23]\n"
    "buffer_load_dwordx4 v253, s[28:31], s58 offen lds\n"
    "s_add_u32 m0, s53, 19456\n"
    "v_mfma_f32_16x16x32_bf16 v[24:27], v[72:75], v[180:183], v[24:27]\n"
    "buffer_load_dwordx4 v250, s[28:31], s58 offen lds\n"
    "s_cmp_eq_u32 s58, s98\n"
    "s_cselect_b32 s58, s94, s58\n"
    "v_mfma_f32_16x16x32_bf16 v[28:31], v[76:79], v[180:183], v[28:31]\n"
    "s_waitcnt vmcnt(12)\n"
    "v_mfma_f32_16x16x32_bf16 v[32:35], v[64:67], v[184:187], v[32:35]\n"
    "ds_read_b64_tr_b16 v[146:147], v240 offset:22528\n"
    "v_mfma_f32_16x16x32_bf16 v[36:39], v[68:71], v[184:187], v[36:39]\n"
    "ds_read_b64_tr_b16 v[150:151], v241 offset:22528\n"
    "v_mfma_f32_16x16x32_bf16 v[40:43], v[72:75], v[184:187], v[40:43]\n"
    "ds_read_b64_tr_b16 v[154:155], v242 offset:22528\n"
    "v_mfma_f32_16x16x32_bf16 v[44:47], v[76:79], v[184:187], v[44:47]\n"
    "ds_read_b64_tr_b16 v[158:159], v243 offset:22528\n"
    "v_mfma_f32_16x16x32_bf16 v[48:51], v[64:67], v[188:191], v[48:51]\n"
    "v_mfma_f32_16x16x32_bf16 v[52:55], v[68:71], v[188:191], v[52:55]\n"
    "v_mfma_f32_16x16x32_bf16 v[56:59], v[72:75], v[188:191], v[56:59]\n"
    "v_mfma_f32_16x16x32_bf16 v[60:63], v[76:79], v[188:191], v[60:63]\n"
    "buffer_load_dwordx4 v[160:163], v244, s[24:27], s72 offen offset:0 sc1\n"
    "buffer_load_dwordx4 v[164:167], v244, s[24:27], s72 offen offset:256 sc1\n"
    "s_waitcnt vmcnt(12)\n"
    "v_mfma_f32_16x16x32_bf16 v[0:3], v[80:83], v[192:195], v[0:3]\n"
    "ds_read_b64_tr_b16 v[64:65], v240 offset:12288\n"
    "v_mfma_f32_16x16x32_bf16 v[4:7], v[84:87], v[192:195], v[4:7]\n"
    "ds_read_b64_tr_b16 v[68:69], v241 offset:12288\n"
    "v_mfma_f32_16x16x32_bf16 v[8:11], v[88:91], v[192:195], v[8:11]\n"
    "ds_read_b64_tr_b16 v[72:73], v242 offset:12288\n"
    "v_mfma_f32_16x16x32_bf16 v[12:15], v[92:95], v[192:195], v[12:15]\n"
    "ds_read_b64_tr_b16 v[76:77], v243 offset:12288\n"
    "v_mfma_f32_16x16x32_bf16 v[16:19], v[80:83], v[196:199], v[16:19]\n"
    "v_mfma_f32_16x16x32_bf16 v[20:23], v[84:87], v[196:199], v[20:23]\n"
    "v_mfma_f32_16x16x32_bf16 v[24:27], v[88:91], v[196:199], v[24:27]\n"
    "v_mfma_f32_16x16x32_bf16 v[28:31], v[92:95], v[196:199], v[28:31]\n"
    "buffer_load_dwordx4 v[168:171], v245, s[24:27], s72 offen offset:0 sc1\n"
    "buffer_load_dwordx4 v[172:175], v245, s[24:27], s72 offen offset:256 sc1\n"
    "s_add_u32 s72, s72, 2048\n"
    "s_cmp_eq_u32 s72, s99\n"
    "s_cselect_b32 s72, s95, s72\n"
    "s_waitcnt vmcnt(10)\n"
    "v_mfma_f32_16x16x32_bf16 v[32:35], v[80:83], v[200:203], v[32:35]\n"
    "ds_read_b64_tr_b16 v[66:67], v240 offset:14336\n"
    "v_mfma_f32_16x16x32_bf16 v[36:39], v[84:87], v[200:203], v[36:39]\n"
    "ds_read_b64_tr_b16 v[70:71], v241 offset:14336\n"
    "v_mfma_f32_16x16x32_bf16 v[40:43], v[88:91], v[200:203], v[40:43]\n"
    "ds_read_b64_tr_b16 v[74:75], v242 offset:14336\n"
    "v_mfma_f32_16x16x32_bf16 v[44:47], v[92:95], v[200:203], v[44:47]\n"
    "ds_read_b64_tr_b16 v[78:79], v243 offset:14336\n"
    "v_mfma_f32_16x16x32_bf16 v[48:51], v[80:83], v[204:207], v[48:51]\n"
    "v_mfma_f32_16x16x32_bf16 v[52:55], v[84:87], v[204:207], v[52:55]\n"
    "v_mfma_f32_16x16x32_bf16 v[56:59], v[88:91], v[204:207], v[56:59]\n"
    "v_mfma_f32_16x16x32_bf16 v[60:63], v[92:95], v[204:207], v[60:63]\n"
    "buffer_load_dwordx4 v[176:179], v244, s[24:27], s72 offen offset:0 sc1\n"
    "buffer_load_dwordx4 v[180:183], v244, s[24:27], s72 offen offset:256 sc1\n"
    "s_waitcnt vmcnt(10)\n"
    "v_mfma_f32_16x16x32_bf16 v[0:3], v[96:99], v[208:211], v[0:3]\n"
    "ds_read_b64_tr_b16 v[80:81], v240 offset:16384\n"
    "v_mfma_f32_16x16x32_bf16 v[4:7], v[100:103], v[208:211], v[4:7]\n"
    "ds_read_b64_tr_b16 v[84:85], v241 offset:16384\n"
    "v_mfma_f32_16x16x32_bf16 v[8:11], v[104:107], v[208:211], v[8:11]\n"
    "ds_read_b64_tr_b16 v[88:89], v242 offset:16384\n"
    "v_mfma_f32_16x16x32_bf16 v[12:15], v[108:111], v[208:211], v[12:15]\n"
    "ds_read_b64_tr_b16 v[92:93], v243 offset:16384\n"
    "v_mfma_f32_16x16x32_bf16 v[16:19], v[96:99], v[212:215], v[16:19]\n"
    "v_mfma_f32_16x16x32_bf16 v[20:23], v[100:103], v[212:215], v[20:23]\n"
    "v_mfma_f32_16x16x32_bf16 v[24:27], v[104:107], v[212:215], v[24:27]\n"
    "v_mfma_f32_16x16x32_bf16 v[28:31], v[108:111], v[212:215], v[28:31]\n"
    "buffer_load_dwordx4 v[184:187], v245, s[24:27], s72 offen offset:0 sc1\n"
    "buffer_load_dwordx4 v[188:191], v245, s[24:27], s72 offen offset:256 sc1\n"
    "s_add_u32 s72, s72, 2048\n"
    "s_cmp_eq_u32 s72, s99\n"
    "s_cselect_b32 s72, s95, s72\n"
    "ds_read_b32 v247, v246 offset:48\n"
    "s_waitcnt vmcnt(8)\n"
    "v_mfma_f32_16x16x32_bf16 v[32:35], v[96:99], v[216:219], v[32:35]\n"
    "ds_read_b64_tr_b16 v[82:83], v240 offset:18432\n"
    "v_mfma_f32_16x16x32_bf16 v[36:39], v[100:103], v[216:219], v[36:39]\n"
    "ds_read_b64_tr_b16 v[86:87], v241 offset:18432\n"
    "v_mfma_f32_16x16x32_bf16 v[40:43], v[104:107], v[216:219], v[40:43]\n"
    "ds_read_b64_tr_b16 v[90:91], v242 offset:18432\n"
    "v_mfma_f32_16x16x32_bf16 v[44:47], v[108:111], v[216:219], v[44:47]\n"
    "ds_read_b64_tr_b16 v[94:95], v243 offset:18432\n"
    "v_mfma_f32_16x16x32_bf16 v[48:51], v[96:99], v[220:223], v[48:51]\n"
    "v_mfma_f32_16x16x32_bf16 v[52:55], v[100:103], v[220:223], v[52:55]\n"
    "v_mfma_f32_16x16x32_bf16 v[56:59], v[104:107], v[220:223], v[56:59]\n"
    "v_mfma_f32_16x16x32_bf16 v[60:63], v[108:111], v[220:223], v[60:63]\n"
    "buffer_load_dwordx4 v[192:195], v244, s[24:27], s72 offen offset:0 sc1\n"
    "buffer_load_dwordx4 v[196:199], v244, s[24:27], s72 offen offset:256 sc1\n"
    "v_mfma_f32_16x16x32_bf16 v[0:3], v[112:115], v[224:227], v[0:3]\n"
    "v_mfma_f32_16x16x32_bf16 v[4:7], v[116:119], v[224:227], v[4:7]\n"
    "v_mfma_f32_16x16x32_bf16 v[8:11], v[120:123], v[224:227], v[8:11]\n"
    "v_mfma_f32_16x16x32_bf16 v[12:15], v[124:127], v[224:227], v[12:15]\n"
    "v_mfma_f32_16x16x32_bf16 v[16:19], v[112:115], v[228:231], v[16:19]\n"
    "v_mfma_f32_16x16x32_bf16 v[20:23], v[116:119], v[228:231], v[20:23]\n"
    "v_mfma_f32_16x16x32_bf16 v[24:27], v[120:123], v[228:231], v[24:27]\n"
    "v_mfma_f32_16x16x32_bf16 v[28:31], v[124:127], v[228:231], v[28:31]\n"
    "buffer_load_dwordx4 v[200:203], v245, s[24:27], s72 offen offset:0 sc1\n"
    "buffer_load_dwordx4 v[204:207], v245, s[24:27], s72 offen offset:256 sc1\n"
    "s_add_u32 s72, s72, 2048\n"
    "s_cmp_eq_u32 s72, s99\n"
    "s_cselect_b32 s72, s95, s72\n"
    "v_mfma_f32_16x16x32_bf16 v[32:35], v[112:115], v[232:235], v[32:35]\n"
    "v_mfma_f32_16x16x32_bf16 v[36:39], v[116:119], v[232:235], v[36:39]\n"
    "v_mfma_f32_16x16x32_bf16 v[40:43], v[120:123], v[232:235], v[40:43]\n"
    "v_mfma_f32_16x16x32_bf16 v[44:47], v[124:127], v[232:235], v[44:47]\n"
    "v_mfma_f32_16x16x32_bf16 v[48:51], v[112:115], v[236:239], v[48:51]\n"
    "v_mfma_f32_16x16x32_bf16 v[52:55], v[116:119], v[236:239], v[52:55]\n"
    "v_mfma_f32_16x16x32_bf16 v[56:59], v[120:123], v[236:239], v[56:59]\n"
    "v_mfma_f32_16x16x32_bf16 v[60:63], v[124:127], v[236:239], v[60:63]\n"
    "buffer_load_dwordx4 v[208:211], v244, s[24:27], s72 offen offset:0 sc1\n"
    "buffer_load_dwordx4 v[212:215], v244, s[24:27], s72 offen offset:256 sc1\n"
    "s_waitcnt vmcnt(12)\n"
    "v_mfma_f32_16x16x32_bf16 v[0:3], v[128:131], v[160:163], v[0:3]\n"
    "v_mfma_f32_16x16x32_bf16 v[4:7], v[132:135], v[160:163], v[4:7]\n"
    "v_mfma_f32_16x16x32_bf16 v[8:11], v[136:139], v[160:163], v[8:11]\n"
    "v_mfma_f32_16x16x32_bf16 v[12:15], v[140:143], v[160:163], v[12:15]\n"
    "v_mfma_f32_16x16x32_bf16 v[16:19], v[128:131], v[164:167], v[16:19]\n"
    "v_mfma_f32_16x16x32_bf16 v[20:23], v[132:135], v[164:167], v[20:23]\n"
    "v_mfma_f32_16x16x32_bf16 v[24:27], v[136:139], v[164:167], v[24:27]\n"
    "v_mfma_f32_16x16x32_bf16 v[28:31], v[140:143], v[164:167], v[28:31]\n"
    "buffer_load_dwordx4 v[216:219], v245, s[24:27], s72 offen offset:0 sc1\n"
    "buffer_load_dwordx4 v[220:223], v245, s[24:27], s72 offen offset:256 sc1\n"
    "s_add_u32 s72, s72, 2048\n"
    "s_cmp_eq_u32 s72, s99\n"
    "s_cselect_b32 s72, s95, s72\n"
    "s_waitcnt vmcnt(12)\n"
    "v_mfma_f32_16x16x32_bf16 v[32:35], v[128:131], v[168:171], v[32:35]\n"
    "v_mfma_f32_16x16x32_bf16 v[36:39], v[132:135], v[168:171], v[36:39]\n"
    "v_mfma_f32_16x16x32_bf16 v[40:43], v[136:139], v[168:171], v[40:43]\n"
    "v_mfma_f32_16x16x32_bf16 v[44:47], v[140:143], v[168:171], v[44:47]\n"
    "v_mfma_f32_16x16x32_bf16 v[48:51], v[128:131], v[172:175], v[48:51]\n"
    "v_mfma_f32_16x16x32_bf16 v[52:55], v[132:135], v[172:175], v[52:55]\n"
    "v_mfma_f32_16x16x32_bf16 v[56:59], v[136:139], v[172:175], v[56:59]\n"
    "v_mfma_f32_16x16x32_bf16 v[60:63], v[140:143], v[172:175], v[60:63]\n"
    "s_waitcnt vmcnt(10)\n"
    "s_waitcnt lgkmcnt(15)\n"
    "v_mfma_f32_16x16x32_bf16 v[0:3], v[144:147], v[176:179], v[0:3]\n"
    "v_mfma_f32_16x16x32_bf16 v[4:7], v[148:151], v[176:179], v[4:7]\n"
    "v_mfma_f32_16x16x32_bf16 v[8:11], v[152:155], v[176:179], v[8:11]\n"
    "v_mfma_f32_16x16x32_bf16 v[12:15], v[156:159], v[176:179], v[12:15]\n"
    "v_mfma_f32_16x16x32_bf16 v[16:19], v[144:147], v[180:183], v[16:19]\n"
    "v_mfma_f32_16x16x32_bf16 v[20:23], v[148:151], v[180:183], v[20:23]\n"
    "v_mfma_f32_16x16x32_bf16 v[24:27], v[152:155], v[180:183], v[24:27]\n"
    "v_mfma_f32_16x16x32_bf16 v[28:31], v[156:159], v[180:183], v[28:31]\n"
    "s_waitcnt vmcnt(8)\n"
    "v_mfma_f32_16x16x32_bf16 v[32:35], v[144:147], v[184:187], v[32:35]\n"
    "v_mfma_f32_16x16x32_bf16 v[36:39], v[148:151], v[184:187], v[36:39]\n"
    "v_mfma_f32_16x16x32_bf16 v[40:43], v[152:155], v[184:187], v[40:43]\n"
    "v_mfma_f32_16x16x32_bf16 v[44:47], v[156:159], v[184:187], v[44:47]\n"
    "v_mfma_f32_16x16x32_bf16 v[48:51], v[144:147], v[188:191], v[48:51]\n"
    "v_mfma_f32_16x16x32_bf16 v[52:55], v[148:151], v[188:191], v[52:55]\n"
    "v_mfma_f32_16x16x32_bf16 v[56:59], v[152:155], v[188:191], v[56:59]\n"
    "v_mfma_f32_16x16x32_bf16 v[60:63], v[156:159], v[188:191], v[60:63]\n"
    "s_waitcnt vmcnt(6)\n"
    "s_waitcnt lgkmcnt(9)\n"
    "v_mfma_f32_16x16x32_bf16 v[0:3], v[64:67], v[192:195], v[0:3]\n"
    "v_mfma_f32_16x16x32_bf16 v[4:7], v[68:71], v[192:195], v[4:7]\n"
    "v_mfma_f32_16x16x32_bf16 v[8:11], v[72:75], v[192:195], v[8:11]\n"
    "v_mfma_f32_16x16x32_bf16 v[12:15], v[76:79], v[192:195], v[12:15]\n"
    "v_mfma_f32_16x16x32_bf16 v[16:19], v[64:67], v[196:199], v[16:19]\n"
    "v_mfma_f32_16x16x32_bf16 v[20:23], v[68:71], v[196:199], v[20:23]\n"
    "v_mfma_f32_16x16x32_bf16 v[24:27], v[72:75], v[196:199], v[24:27]\n"
    "v_mfma_f32_16x16x32_bf16 v[28:31], v[76:79], v[196:199], v[28:31]\n"
    "s_waitcnt vmcnt(4)\n"
    "v_mfma_f32_16x16x32_bf16 v[32:35], v[64:67], v[200:203], v[32:35]\n"
    "v_mfma_f32_16x16x32_bf16 v[36:39], v[68:71], v[200:203], v[36:39]\n"
    "v_mfma_f32_16x16x32_bf16 v[40:43], v[72:75], v[200:203], v[40:43]\n"
    "v_mfma_f32_16x16x32_bf16 v[44:47], v[76:79], v[200:203], v[44:47]\n"
    "v_mfma_f32_16x16x32_bf16 v[48:51], v[64:67], v[204:207], v[48:51]\n"
    "v_mfma_f32_16x16x32_bf16 v[52:55], v[68:71], v[204:207], v[52:55]\n"
    "v_mfma_f32_16x16x32_bf16 v[56:59], v[72:75], v[204:207], v[56:59]\n"
    "v_mfma_f32_16x16x32_bf16 v[60:63], v[76:79], v[204:207], v[60:63]\n"
    "s_waitcnt vmcnt(2)\n"
    "s_waitcnt lgkmcnt(0)\n"
    "v_mfma_f32_16x16x32_bf16 v[0:3], v[80:83], v[208:211], v[0:3]\n"
    "v_mfma_f32_16x16x32_bf16 v[4:7], v[84:87], v[208:211], v[4:7]\n"
    "v_mfma_f32_16x16x32_bf16 v[8:11], v[88:91], v[208:211], v[8:11]\n"
    "v_mfma_f32_16x16x32_bf16 v[12:15], v[92:95], v[208:211], v[12:15]\n"
    "v_mfma_f32_16x16x32_bf16 v[16:19], v[80:83], v[212:215], v[16:19]\n"
    "v_mfma_f32_16x16x32_bf16 v[20:23], v[84:87], v[212:215], v[20:23]\n"
    "v_mfma_f32_16x16x32_bf16 v[24:27], v[88:91], v[212:215], v[24:27]\n"
    "v_mfma_f32_16x16x32_bf16 v[28:31], v[92:95], v[212:215], v[28:31]\n"
    "s_waitcnt vmcnt(0)\n"
    "v_readfirstlane_b32 s91, v247\n"
    "s_cmp_ge_u32 s91, 2\n"
    "s_cbranch_scc0 .Lgm_lc_22\n"
    ".Lgm_lcb_23:\n"
    "v_mfma_f32_16x16x32_bf16 v[32:35], v[80:83], v[216:219], v[32:35]\n"
    "ds_write_b128 v252, v[0:3] offset:0\n"
    "v_mfma_f32_16x16x32_bf16 v[36:39], v[84:87], v[216:219], v[36:39]\n"
    "ds_write_b128 v252, v[4:7] offset:1024\n"
    "v_mfma_f32_16x16x32_bf16 v[40:43], v[88:91], v[216:219], v[40:43]\n"
    "ds_write_b128 v252, v[8:11] offset:2048\n"
    "v_mfma_f32_16x16x32_bf16 v[44:47], v[92:95], v[216:219], v[44:47]\n"
    "ds_write_b128 v252, v[12:15] offset:3072\n"
    "v_mfma_f32_16x16x32_bf16 v[48:51], v[80:83], v[220:223], v[48:51]\n"
    "ds_write_b128 v252, v[16:19] offset:4096\n"
    "v_mfma_f32_16x16x32_bf16 v[52:55], v[84:87], v[220:223], v[52:55]\n"
    "ds_write_b128 v252, v[20:23] offset:5120\n"
    "v_mfma_f32_16x16x32_bf16 v[56:59], v[88:91], v[220:223], v[56:59]\n"
    "ds_write_b128 v252, v[24:27] offset:6144\n"
    "v_mfma_f32_16x16x32_bf16 v[60:63], v[92:95], v[220:223], v[60:63]\n"
    "ds_write_b128 v252, v[28:31] offset:7168\n"
    "ds_write_b128 v252, v[32:35] offset:8192\n"
    "ds_write_b128 v252, v[36:39] offset:9216\n"
    "ds_write_b128 v252, v[40:43] offset:10240\n"
    "ds_write_b128 v252, v[44:47] offset:11264\n"
    "ds_write_b128 v252, v[48:51] offset:12288\n"
    "ds_write_b128 v252, v[52:55] offset:13312\n"
    "ds_write_b128 v252, v[56:59] offset:14336\n"
    "ds_write_b128 v252, v[60:63] offset:15360\n"
    "s_mov_b64 exec, 1\n"
    "ds_add_u32 v249, v248 offset:36\n"
    "s_mov_b64 exec, -1\n"
    "s_endpgm\n"
    ".Lgm_roleA_1:\n"
    "v_lshlrev_b32 v252, 4, v255\n"
    "v_and_b32 v236, 31, v255\n"
    "v_lshrrev_b32 v237, 5, v255\n"
    "v_bfe_u32 v234, v255, 2, 2\n"
    "v_mov_b32 v249, 158720\n"
    "v_mov_b32 v248, 1\n"
    "s_lshl_b32 s63, s47, 2\n"
    "s_add_u32 s63, s63, s59\n"
    "s_lshl_b32 s64, s47, 4\n"
    "s_add_u32 s64, s64, s48\n"
    "s_lshl_b32 s64, s64, 7\n"
    "s_add_u32 s64, s64, 128\n"
    "s_add_u32 s73, s64, 32768\n"
    "s_mul_i32 s84, s46, 37\n"
    "s_lshr_b32 s84, s84, 8\n"
    "s_mul_i32 s85, s84, 7\n"
    "s_sub_u32 s68, s46, s85\n"
    "s_lshl_b32 s67, s45, 2\n"
    "s_add_u32 s67, s67, s84\n"
    "s_mov_b32 s82, 0x3fb8aa3b\n"
    "s_mov_b32 s83, 0x3fb504f3\n"
    "v_lshl_add_u32 v2, v237, 3, v234\n"
    "v_lshlrev_b32 v2, 8, v2\n"
    "v_and_b32 v4, 16, v255\n"
    "v_lshl_add_u32 v2, v4, 1, v2\n"
    "v_and_b32 v4, 1, v255\n"
    "v_lshl_add_u32 v2, v4, 4, v2\n"
    "v_and_b32 v4, 2, v255\n"
    "v_lshl_add_u32 v233, v4, 2, v2\n"
    "s_lshl_b32 s84, s59, 12\n"
    "v_bfe_u32 v2, v255, 1, 3\n"
    "v_xor_b32 v2, v2, v237\n"
    "v_lshlrev_b32 v2, 4, v2\n"
    "v_lshl_add_u32 v2, v236, 7, v2\n"
    "v_add_u32 v224, s84, v2\n"
    "v_xor_b32 v225, 32, v224\n"
    "v_xor_b32 v226, 64, v224\n"
    "v_xor_b32 v227, 0x60, v224\n"
    "s_add_u32 s84, s60, 0\n"
    "v_xor_b32 v116, s84, v234\n"
    "v_lshl_add_u32 v116, v116, 6, v233\n"
    "s_add_u32 s84, s60, 1\n"
    "v_xor_b32 v117, s84, v234\n"
    "v_lshl_add_u32 v117, v117, 6, v233\n"
    "s_add_u32 s84, s60, 2\n"
    "v_xor_b32 v118, s84, v234\n"
    "v_lshl_add_u32 v118, v118, 6, v233\n"
    "s_and_b32 s84, s46, 1\n"
    "s_lshl_b32 s65, s45, 1\n"
    "s_add_u32 s65, s65, s84\n"
    "s_lshr_b32 s66, s46, 1\n"
    "s_mul_i32 s69, s45, 7\n"
    "s_mul_i32 s84, s84, 3\n"
    "s_add_u32 s69, s69, s84\n"
    "v_mov_b64 v[8:9], 0\n"
    "v_mov_b64 v[10:11], 0\n"
    "v_mov_b64 v[12:13], 0\n"
    "v_mov_b64 v[14:15], 0\n"
    "v_mov_b64 v[48:49], 0\n"
    "v_mov_b64 v[50:51], 0\n"
    "v_mov_b64 v[52:53], 0\n"
    "v_mov_b64 v[54:55], 0\n"
    "v_mov_b64 v[56:57], 0\n"
    "v_mov_b64 v[58:59], 0\n"
    "v_mov_b64 v[64:65], 0\n"
    "v_mov_b64 v[66:67], 0\n"
    "v_mov_b64 v[68:69], 0\n"
    "v_mov_b64 v[70:71], 0\n"
    "v_mov_b64 v[72:73], 0\n"
    "v_mov_b64 v[74:75], 0\n"
    "v_mov_b64 v[128:129], 0\n"
    "v_mov_b64 v[130:131], 0\n"
    "v_mov_b64 v[132:133], 0\n"
    "v_mov_b64 v[134:135], 0\n"
    "v_mov_b64 v[136:137], 0\n"
    "v_mov_b64 v[138:139], 0\n"
    "v_mov_b64 v[140:141], 0\n"
    "v_mov_b64 v[142:143], 0\n"
    "v_mov_b64 v[144:145], 0\n"
    "v_mov_b64 v[146:147], 0\n"
    "v_mov_b64 v[148:149], 0\n"
    "v_mov_b64 v[150:151], 0\n"
    "v_mov_b64 v[152:153], 0\n"
    "v_mov_b64 v[154:155], 0\n"
    "v_mov_b64 v[156:157], 0\n"
    "v_mov_b64 v[158:159], 0\n"
    "v_mov_b64 v[160:161], 0\n"
    "v_mov_b64 v[162:163], 0\n"
    "v_mov_b64 v[164:165], 0\n"
    "v_mov_b64 v[166:167], 0\n"
    "v_mov_b64 v[168:169], 0\n"
    "v_mov_b64 v[170:171], 0\n"
    "v_mov_b64 v[172:173], 0\n"
    "v_mov_b64 v[174:175], 0\n"
    "ds_write_b128 v249, v[8:11] offset:0\n"
    "ds_write_b128 v249, v[8:11] offset:16\n"
    "ds_write_b128 v249, v[8:11] offset:32\n"
    "ds_write_b128 v249, v[8:11] offset:48\n"
    "ds_write_b128 v249, v[8:11] offset:80\n"
    "v_and_b32 v0, 19, v236\n"
    "v_and_b32 v1, 4, v236\n"
    "v_lshl_or_b32 v0, v1, 1, v0\n"
    "v_bfe_u32 v1, v236, 3, 1\n"
    "v_lshl_or_b32 v0, v1, 2, v0\n"
    "v_lshlrev_b32 v0, 4, v0\n"
    "v_lshl_add_u32 v1, v237, 9, v0\n"
    "v_mul_u32_u24 v2, 7168, v237\n"
    "v_add_u32 v2, v2, v0\n"
    "s_mul_i32 s84, s63, 86016\n"
    "s_add_u32 s84, s84, 262144\n"
    "s_add_u32 s85, s62, 0\n"
    "s_lshl_b32 s51, s85, 11\n"
    "s_lshl_b32 s52, s85, 9\n"
    "s_add_u32 s52, s52, 43008\n"
    "s_cmp_ge_u32 s85, 28\n"
    "s_cselect_b32 s51, s52, s51\n"
    "s_cselect_b64 vcc, -1, 0\n"
    "s_movk_i32 s52, 1024\n"
    "s_cselect_b32 s52, 14336, s52\n"
    "v_cndmask_b32 v80, v1, v2, vcc\n"
    "s_add_u32 s51, s51, s84\n"
    "s_add_u32 s52, s52, s51\n"
    "s_cmp_lt_u32 s85, 14\n"
    "s_cselect_b64 vcc, -1, 0\n"
    "v_cndmask_b32 v80, v80, v252, vcc\n"
    "s_add_u32 s85, s62, 1\n"
    "s_lshl_b32 s55, s85, 11\n"
    "s_lshl_b32 s56, s85, 9\n"
    "s_add_u32 s56, s56, 43008\n"
    "s_cmp_ge_u32 s85, 28\n"
    "s_cselect_b32 s55, s56, s55\n"
    "s_cselect_b64 vcc, -1, 0\n"
    "s_movk_i32 s56, 1024\n"
    "s_cselect_b32 s56, 14336, s56\n"
    "v_cndmask_b32 v81, v1, v2, vcc\n"
    "s_add_u32 s55, s55, s84\n"
    "s_add_u32 s56, s56, s55\n"
    "s_cmp_lt_u32 s85, 14\n"
    "s_cselect_b64 vcc, -1, 0\n"
    "v_cndmask_b32 v81, v81, v252, vcc\n"
    "s_add_u32 s85, s62, 2\n"
    "s_lshl_b32 s57, s85, 11\n"
    "s_lshl_b32 s58, s85, 9\n"
    "s_add_u32 s58, s58, 43008\n"
    "s_cmp_ge_u32 s85, 28\n"
    "s_cselect_b32 s57, s58, s57\n"
    "s_cselect_b64 vcc, -1, 0\n"
    "s_movk_i32 s58, 1024\n"
    "s_cselect_b32 s58, 14336, s58\n"
    "v_cndmask_b32 v82, v1, v2, vcc\n"
    "s_add_u32 s57, s57, s84\n"
    "s_add_u32 s58, s58, s57\n"
    "s_cmp_lt_u32 s85, 14\n"
    "s_cselect_b64 vcc, -1, 0\n"
    "v_cndmask_b32 v82, v82, v252, vcc\n"
    "s_waitcnt lgkmcnt(0)\n"
    "v_and_b32 v1, 1, v255\n"
    "v_mad_u32_u24 v2, v1, 3, s69\n"
    "v_lshrrev_b32 v2, 2, v2\n"
    "s_mul_i32 s84, s65, 28\n"
    "v_mad_u32_u24 v3, v1, 27, s84\n"
    "v_lshrrev_b32 v3, 5, v3\n"
    "v_add_u32 v3, 28, v3\n"
    "v_and_b32 v5, 2, v255\n"
    "v_cmp_eq_u32 vcc, 0, v5\n"
    "v_add_u32 v4, 14, v2\n"
    "s_lshl_b32 s85, s44, 2\n"
    "v_cndmask_b32 v3, v3, v4, vcc\n"
    "v_cmp_gt_u32 vcc, 16, v255\n"
    "v_lshrrev_b32 v4, 2, v255\n"
    "v_add_u32 v4, s85, v4\n"
    "v_mov_b32 v5, s66\n"
    "v_cndmask_b32 v3, v2, v3, vcc\n"
    "v_cndmask_b32 v4, v5, v4, vcc\n"
    "v_mul_u32_u24 v3, 43, v3\n"
    "v_lshrrev_b32 v3, 7, v3\n"
    "v_lshl_add_u32 v3, v4, 4, v3\n"
    "v_lshlrev_b32 v124, 7, v3\n"
    "v_add_u32 v124, 128, v124\n"
    "v_add_u32 v126, s85, v249\n"
    "s_lshl_b32 s85, s69, 9\n"
    "v_add_u32 v105, s85, v252\n"
    "v_add_u32 v106, 28672, v105\n"
    "v_add_u32 v1, s84, v236\n"
    "v_mul_u32_u24 v2, 7168, v237\n"
    "v_lshl_add_u32 v1, v1, 4, v2\n"
    "v_add_u32 v107, 57344, v1\n"
    "v_add_u32 v108, 71680, v1\n"
    "v_cmp_eq_u32 vcc, 28, v236\n"
    "s_and_b32 s85, s65, 1\n"
    "s_xor_b32 s86, s85, 1\n"
    "v_mov_b32 v1, 0x3f803f80\n"
    "v_cndmask_b32 v125, 0, v1, vcc\n"
    "v_or_b32 v1, s86, v237\n"
    "v_sub_u32 v109, 0, v1\n"
    "v_xor_b32 v1, 1, v237\n"
    "v_or_b32 v1, s85, v1\n"
    "v_sub_u32 v110, 0, v1\n"
    "s_mul_i32 s71, s44, 1376256\n"
    "s_add_u32 s71, s71, 262144\n"
    "s_mov_b64 s[76:77], s[24:25]\n"
    "s_add_u32 s78, s71, 1376256\n"
    "s_mov_b32 s79, s27\n"
    "s_mov_b32 s54, s71\n"
    "s_and_b32 s87, s66, 3\n"
    "s_mul_i32 s87, s87, 344064\n"
    "s_add_u32 s71, s71, s87\n"
    "s_mov_b32 s80, 0x0fffffff\n"
    "s_mov_b32 s81, 0x0fffffff\n"
    "v_mov_b32 v104, 0xdddddddd\n"
    "s_lshl_b32 s88, s66, 2\n"
    "s_add_u32 s96, s44, 1\n"
    "s_and_b32 s96, s96, 3\n"
    "s_add_u32 s96, s96, s88\n"
    "s_mul_i32 s96, s96, 86016\n"
    "s_add_u32 s96, s96, 262144\n"
    "s_add_u32 s97, s44, 2\n"
    "s_and_b32 s97, s97, 3\n"
    "s_add_u32 s97, s97, s88\n"
    "s_mul_i32 s97, s97, 86016\n"
    "s_add_u32 s97, s97, 262144\n"
    "s_add_u32 s98, s44, 3\n"
    "s_and_b32 s98, s98, 3\n"
    "s_add_u32 s98, s98, s88\n"
    "s_mul_i32 s98, s98, 86016\n"
    "s_add_u32 s98, s98, 262144\n"
    "s_add_u32 s99, s44, 4\n"
    "s_and_b32 s99, s99, 3\n"
    "s_add_u32 s99, s99, s88\n"
    "s_mul_i32 s99, s99, 86016\n"
    "s_add_u32 s99, s99, 262144\n"
    "s_mov_b32 s94, 0\n"
    "s_mov_b32 s70, 7\n"
    "s_cmp_ge_u32 s46, 28\n"
    "s_cbranch_scc1 .Lgm_spec_27\n"
    "s_cmp_eq_u32 s48, 9\n"
    "s_cbranch_scc1 .Lgm_tvv_24\n"
    "s_cmp_gt_u32 s48, 9\n"
    "s_cbranch_scc0 .Lgm_vvv_25\n"
    "v_add_u32 v112, s94, v224\n"
    "v_add_u32 v113, s94, v225\n"
    "v_add_u32 v114, s94, v226\n"
    "v_add_u32 v115, s94, v227\n"
    "v_add_u32 v120, s94, v116\n"
    "v_add_u32 v121, s94, v117\n"
    "v_add_u32 v122, s94, v118\n"
    ".Lgm_s1_29:\n"
    "s_waitcnt lgkmcnt(0)\n"
    "s_barrier\n"
    "v_mfma_f32_32x32x16_bf16 v[128:143], v[8:11], v[48:51], v[128:143]\n"
    "ds_read_b128 v[0:3], v112\n"
    "ds_read_b64_tr_b16 v[16:17], v120 offset:16384\n"
    "ds_read_b64_tr_b16 v[18:19], v120 offset:17408\n"
    "s_add_u32 s94, s94, 49152\n"
    "v_mfma_f32_32x32x16_bf16 v[144:159], v[8:11], v[52:55], v[144:159]\n"
    "ds_read_b64_tr_b16 v[20:21], v121 offset:16384\n"
    "ds_read_b64_tr_b16 v[22:23], v121 offset:17408\n"
    "ds_read_b64_tr_b16 v[24:25], v122 offset:16384\n"
    "s_cmp_eq_u32 s94, 147456\n"
    "v_mfma_f32_32x32x16_bf16 v[160:175], v[8:11], v[56:59], v[160:175]\n"
    "ds_read_b64_tr_b16 v[26:27], v122 offset:17408\n"
    "ds_read_b128 v[4:7], v113\n"
    "ds_read_b64_tr_b16 v[32:33], v120 offset:20480\n"
    "s_cselect_b32 s94, 0, s94\n"
    "v_mfma_f32_32x32x16_bf16 v[128:143], v[12:15], v[64:67], v[128:143]\n"
    "ds_read_b64_tr_b16 v[34:35], v120 offset:21504\n"
    "ds_read_b64_tr_b16 v[36:37], v121 offset:20480\n"
    "ds_read_b64_tr_b16 v[38:39], v121 offset:21504\n"
    "v_mfma_f32_32x32x16_bf16 v[144:159], v[12:15], v[68:71], v[144:159]\n"
    "ds_read_b64_tr_b16 v[40:41], v122 offset:20480\n"
    "ds_read_b64_tr_b16 v[42:43], v122 offset:21504\n"
    "ds_read_b128 v[8:11], v114\n"
    "v_mfma_f32_32x32x16_bf16 v[160:175], v[12:15], v[72:75], v[160:175]\n"
    "ds_read_b64_tr_b16 v[48:49], v120 offset:24576\n"
    "ds_read_b64_tr_b16 v[50:51], v120 offset:25600\n"
    "ds_read_b64_tr_b16 v[52:53], v121 offset:24576\n"
    "s_waitcnt lgkmcnt(11)\n"
    "v_mfma_f32_32x32x16_bf16 v[128:143], v[0:3], v[16:19], v[128:143]\n"
    "ds_read_b64_tr_b16 v[54:55], v121 offset:25600\n"
    "ds_read_b64_tr_b16 v[56:57], v122 offset:24576\n"
    "ds_read_b64_tr_b16 v[58:59], v122 offset:25600\n"
    "v_mfma_f32_32x32x16_bf16 v[144:159], v[0:3], v[20:23], v[144:159]\n"
    "ds_read_b128 v[12:15], v115\n"
    "ds_read_b64_tr_b16 v[64:65], v120 offset:28672\n"
    "ds_read_b64_tr_b16 v[66:67], v120 offset:29696\n"
    "v_mfma_f32_32x32x16_bf16 v[160:175], v[0:3], v[24:27], v[160:175]\n"
    "ds_read_b64_tr_b16 v[68:69], v121 offset:28672\n"
    "ds_read_b64_tr_b16 v[70:71], v121 offset:29696\n"
    "ds_read_b64_tr_b16 v[72:73], v122 offset:28672\n"
    "ds_read_b64_tr_b16 v[74:75], v122 offset:29696\n"
    "s_waitcnt lgkmcnt(14)\n"
    "v_mfma_f32_32x32x16_bf16 v[128:143], v[4:7], v[32:35], v[128:143]\n"
    "v_add_u32 v112, s94, v224\n"
    "v_add_u32 v113, s94, v225\n"
    "v_add_u32 v114, s94, v226\n"
    "v_add_u32 v115, s94, v227\n"
    "v_mfma_f32_32x32x16_bf16 v[144:159], v[4:7], v[36:39], v[144:159]\n"
    "v_add_u32 v120, s94, v116\n"
    "v_add_u32 v121, s94, v117\n"
    "v_add_u32 v122, s94, v118\n"
    "s_sub_u32 s70, s70, 1\n"
    "s_cmp_lg_u32 s70, 0\n"
    "v_mfma_f32_32x32x16_bf16 v[160:175], v[4:7], v[40:43], v[160:175]\n"
    "s_cbranch_scc1 .Lgm_s1_29\n"
    "s_waitcnt lgkmcnt(0)\n"
    "v_mfma_f32_32x32x16_bf16 v[128:143], v[8:11], v[48:51], v[128:143]\n"
    "v_mfma_f32_32x32x16_bf16 v[128:143], v[12:15], v[64:67], v[128:143]\n"
    "v_mfma_f32_32x32x16_bf16 v[144:159], v[8:11], v[52:55], v[144:159]\n"
    "v_mfma_f32_32x32x16_bf16 v[144:159], v[12:15], v[68:71], v[144:159]\n"
    "v_mfma_f32_32x32x16_bf16 v[160:175], v[8:11], v[56:59], v[160:175]\n"
    "v_mfma_f32_32x32x16_bf16 v[160:175], v[12:15], v[72:75], v[160:175]\n"
    "s_branch .Lgm_e1_26\n"
    ".Lgm_tvv_24:\n"
    "v_add_u32 v112, s94, v224\n"
    "v_add_u32 v113, s94, v225\n"
    "v_add_u32 v114, s94, v226\n"
    "v_add_u32 v115, s94, v227\n"
    "v_add_u32 v120, s94, v116\n"
    "v_add_u32 v121, s94, v117\n"
    "v_add_u32 v122, s94, v118\n"
    ".Lgm_s1_30:\n"
    "s_waitcnt lgkmcnt(0)\n"
    "s_barrier\n"
    "v_mfma_f32_32x32x16_bf16 v[128:143], v[48:51], v[8:11], v[128:143]\n"
    "ds_read_b128 v[0:3], v112\n"
    "ds_read_b64_tr_b16 v[16:17], v120 offset:16384\n"
    "ds_read_b64_tr_b16 v[18:19], v120 offset:17408\n"
    "s_add_u32 s94, s94, 49152\n"
    "v_mfma_f32_32x32x16_bf16 v[144:159], v[8:11], v[52:55], v[144:159]\n"
    "ds_read_b64_tr_b16 v[20:21], v121 offset:16384\n"
    "ds_read_b64_tr_b16 v[22:23], v121 offset:17408\n"
    "ds_read_b64_tr_b16 v[24:25], v122 offset:16384\n"
    "s_cmp_eq_u32 s94, 147456\n"
    "v_mfma_f32_32x32x16_bf16 v[160:175], v[8:11], v[56:59], v[160:175]\n"
    "ds_read_b64_tr_b16 v[26:27], v122 offset:17408\n"
    "ds_read_b128 v[4:7], v113\n"
    "ds_read_b64_tr_b16 v[32:33], v120 offset:20480\n"
    "s_cselect_b32 s94, 0, s94\n"
    "v_mfma_f32_32x32x16_bf16 v[128:143], v[64:67], v[12:15], v[128:143]\n"
    "ds_read_b64_tr_b16 v[34:35], v120 offset:21504\n"
    "ds_read_b64_tr_b16 v[36:37], v121 offset:20480\n"
    "ds_read_b64_tr_b16 v[38:39], v121 offset:21504\n"
    "v_mfma_f32_32x32x16_bf16 v[144:159], v[12:15], v[68:71], v[144:159]\n"
    "ds_read_b64_tr_b16 v[40:41], v122 offset:20480\n"
    "ds_read_b64_tr_b16 v[42:43], v122 offset:21504\n"
    "ds_read_b128 v[8:11], v114\n"
    "v_mfma_f32_32x32x16_bf16 v[160:175], v[12:15], v[72:75], v[160:175]\n"
    "ds_read_b64_tr_b16 v[48:49], v120 offset:24576\n"
    "ds_read_b64_tr_b16 v[50:51], v120 offset:25600\n"
    "ds_read_b64_tr_b16 v[52:53], v121 offset:24576\n"
    "s_waitcnt lgkmcnt(11)\n"
    "v_mfma_f32_32x32x16_bf16 v[128:143], v[16:19], v[0:3], v[128:143]\n"
    "ds_read_b64_tr_b16 v[54:55], v121 offset:25600\n"
    "ds_read_b64_tr_b16 v[56:57], v122 offset:24576\n"
    "ds_read_b64_tr_b16 v[58:59], v122 offset:25600\n"
    "v_mfma_f32_32x32x16_bf16 v[144:159], v[0:3], v[20:23], v[144:159]\n"
    "ds_read_b128 v[12:15], v115\n"
    "ds_read_b64_tr_b16 v[64:65], v120 offset:28672\n"
    "ds_read_b64_tr_b16 v[66:67], v120 offset:29696\n"
    "v_mfma_f32_32x32x16_bf16 v[160:175], v[0:3], v[24:27], v[160:175]\n"
    "ds_read_b64_tr_b16 v[68:69], v121 offset:28672\n"
    "ds_read_b64_tr_b16 v[70:71], v121 offset:29696\n"
    "ds_read_b64_tr_b16 v[72:73], v122 offset:28672\n"
    "ds_read_b64_tr_b16 v[74:75], v122 offset:29696\n"
    "s_waitcnt lgkmcnt(14)\n"
    "v_mfma_f32_32x32x16_bf16 v[128:143], v[32:35], v[4:7], v[128:143]\n"
    "v_add_u32 v112, s94, v224\n"
    "v_add_u32 v113, s94, v225\n"
    "v_add_u32 v114, s94, v226\n"
    "v_add_u32 v115, s94, v227\n"
    "v_mfma_f32_32x32x16_bf16 v[144:159], v[4:7], v[36:39], v[144:159]\n"
    "v_add_u32 v120, s94, v116\n"
    "v_add_u32 v121, s94, v117\n"
    "v_add_u32 v122, s94, v118\n"
    "s_sub_u32 s70, s70, 1\n"
    "s_cmp_lg_u32 s70, 0\n"
    "v_mfma_f32_32x32x16_bf16 v[160:175], v[4:7], v[40:43], v[160:175]\n"
    "s_cbranch_scc1 .Lgm_s1_30\n"
    "s_waitcnt lgkmcnt(0)\n"
    "v_mfma_f32_32x32x16_bf16 v[128:143], v[48:51], v[8:11], v[128:143]\n"
    "v_mfma_f32_32x32x16_bf16 v[128:143], v[64:67], v[12:15], v[128:143]\n"
    "v_mfma_f32_32x32x16_bf16 v[144:159], v[8:11], v[52:55], v[144:159]\n"
    "v_mfma_f32_32x32x16_bf16 v[144:159], v[12:15], v[68:71], v[144:159]\n"
    "v_mfma_f32_32x32x16_bf16 v[160:175], v[8:11], v[56:59], v[160:175]\n"
    "v_mfma_f32_32x32x16_bf16 v[160:175], v[12:15], v[72:75], v[160:175]\n"
    "s_branch .Lgm_e1_26\n"
    ".Lgm_vvv_25:\n"
    "v_add_u32 v112, s94, v224\n"
    "v_add_u32 v113, s94, v225\n"
    "v_add_u32 v114, s94, v226\n"
    "v_add_u32 v115, s94, v227\n"
    "v_add_u32 v120, s94, v116\n"
    "v_add_u32 v121, s94, v117\n"
    "v_add_u32 v122, s94, v118\n"
    ".Lgm_s1_31:\n"
    "s_waitcnt lgkmcnt(0)\n"
    "s_barrier\n"
    "v_mfma_f32_32x32x16_bf16 v[128:143], v[48:51], v[8:11], v[128:143]\n"
    "ds_read_b128 v[0:3], v112\n"
    "ds_read_b64_tr_b16 v[16:17], v120 offset:16384\n"
    "ds_read_b64_tr_b16 v[18:19], v120 offset:17408\n"
    "s_add_u32 s94, s94, 49152\n"
    "v_mfma_f32_32x32x16_bf16 v[144:159], v[52:55], v[8:11], v[144:159]\n"
    "ds_read_b64_tr_b16 v[20:21], v121 offset:16384\n"
    "ds_read_b64_tr_b16 v[22:23], v121 offset:17408\n"
    "ds_read_b64_tr_b16 v[24:25], v122 offset:16384\n"
    "s_cmp_eq_u32 s94, 147456\n"
    "v_mfma_f32_32x32x16_bf16 v[160:175], v[56:59], v[8:11], v[160:175]\n"
    "ds_read_b64_tr_b16 v[26:27], v122 offset:17408\n"
    "ds_read_b128 v[4:7], v113\n"
    "ds_read_b64_tr_b16 v[32:33], v120 offset:20480\n"
    "s_cselect_b32 s94, 0, s94\n"
    "v_mfma_f32_32x32x16_bf16 v[128:143], v[64:67], v[12:15], v[128:143]\n"
    "ds_read_b64_tr_b16 v[34:35], v120 offset:21504\n"
    "ds_read_b64_tr_b16 v[36:37], v121 offset:20480\n"
    "ds_read_b64_tr_b16 v[38:39], v121 offset:21504\n"
    "v_mfma_f32_32x32x16_bf16 v[144:159], v[68:71], v[12:15], v[144:159]\n"
    "ds_read_b64_tr_b16 v[40:41], v122 offset:20480\n"
    "ds_read_b64_tr_b16 v[42:43], v122 offset:21504\n"
    "ds_read_b128 v[8:11], v114\n"
    "v_mfma_f32_32x32x16_bf16 v[160:175], v[72:75], v[12:15], v[160:175]\n"
    "ds_read_b64_tr_b16 v[48:49], v120 offset:24576\n"
    "ds_read_b64_tr_b16 v[50:51], v120 offset:25600\n"
    "ds_read_b64_tr_b16 v[52:53], v121 offset:24576\n"
    "s_waitcnt lgkmcnt(11)\n"
    "v_mfma_f32_32x32x16_bf16 v[128:143], v[16:19], v[0:3], v[128:143]\n"
    "ds_read_b64_tr_b16 v[54:55], v121 offset:25600\n"
    "ds_read_b64_tr_b16 v[56:57], v122 offset:24576\n"
    "ds_read_b64_tr_b16 v[58:59], v122 offset:25600\n"
    "v_mfma_f32_32x32x16_bf16 v[144:159], v[20:23], v[0:3], v[144:159]\n"
    "ds_read_b128 v[12:15], v115\n"
    "ds_read_b64_tr_b16 v[64:65], v120 offset:28672\n"
    "ds_read_b64_tr_b16 v[66:67], v120 offset:29696\n"
    "v_mfma_f32_32x32x16_bf16 v[160:175], v[24:27], v[0:3], v[160:175]\n"
    "ds_read_b64_tr_b16 v[68:69], v121 offset:28672\n"
    "ds_read_b64_tr_b16 v[70:71], v121 offset:29696\n"
    "ds_read_b64_tr_b16 v[72:73], v122 offset:28672\n"
    "ds_read_b64_tr_b16 v[74:75], v122 offset:29696\n"
    "s_waitcnt lgkmcnt(14)\n"
    "v_mfma_f32_32x32x16_bf16 v[128:143], v[32:35], v[4:7], v[128:143]\n"
    "v_add_u32 v112, s94, v224\n"
    "v_add_u32 v113, s94, v225\n"
    "v_add_u32 v114, s94, v226\n"
    "v_add_u32 v115, s94, v227\n"
    "v_mfma_f32_32x32x16_bf16 v[144:159], v[36:39], v[4:7], v[144:159]\n"
    "v_add_u32 v120, s94, v116\n"
    "v_add_u32 v121, s94, v117\n"
    "v_add_u32 v122, s94, v118\n"
    "s_sub_u32 s70, s70, 1\n"
    "s_cmp_lg_u32 s70, 0\n"
    "v_mfma_f32_32x32x16_bf16 v[160:175], v[40:43], v[4:7], v[160:175]\n"
    "s_cbranch_scc1 .Lgm_s1_31\n"
    "s_waitcnt lgkmcnt(0)\n"
    "v_mfma_f32_32x32x16_bf16 v[128:143], v[48:51], v[8:11], v[128:143]\n"
    "v_mfma_f32_32x32x16_bf16 v[128:143], v[64:67], v[12:15], v[128:143]\n"
    "v_mfma_f32_32x32x16_bf16 v[144:159], v[52:55], v[8:11], v[144:159]\n"
    "v_mfma_f32_32x32x16_bf16 v[144:159], v[68:71], v[12:15], v[144:159]\n"
    "v_mfma_f32_32x32x16_bf16 v[160:175], v[56:59], v[8:11], v[160:175]\n"
    "v_mfma_f32_32x32x16_bf16 v[160:175], v[72:75], v[12:15], v[160:175]\n"
    ".Lgm_e1_26:\n"
    "s_setprio 2\n"
    "ds_read_b64 v[250:251], v249 offset:64\n"
    "s_mov_b32 s86, 0x3e8b97f4\n"
    "s_cmp_lt_u32 s62, 14\n"
    "s_cbranch_scc0 .Lgm_nq_32\n"
    "s_nop 2\n"
    "v_mul_f32 v128, s86, v128\n"
    "v_mul_f32 v129, s86, v129\n"
    "v_mul_f32 v130, s86, v130\n"
    "v_mul_f32 v131, s86, v131\n"
    "v_mul_f32 v132, s86, v132\n"
    "v_mul_f32 v133, s86, v133\n"
    "v_mul_f32 v134, s86, v134\n"
    "v_mul_f32 v135, s86, v135\n"
    "v_mul_f32 v136, s86, v136\n"
    "v_mul_f32 v137, s86, v137\n"
    "v_mul_f32 v138, s86, v138\n"
    "v_mul_f32 v139, s86, v139\n"
    "v_mul_f32 v140, s86, v140\n"
    "v_mul_f32 v141, s86, v141\n"
    "v_mul_f32 v142, s86, v142\n"
    "v_mul_f32 v143, s86, v143\n"
    ".Lgm_nq_32:\n"
    "s_nop 2\n"
    "v_cvt_pk_bf16_f32 v192, v128, v129\n"
    "v_cvt_pk_bf16_f32 v193, v130, v131\n"
    "v_cvt_pk_bf16_f32 v194, v132, v133\n"
    "v_cvt_pk_bf16_f32 v195, v134, v135\n"
    "buffer_store_dwordx4 v[192:195], v80, s[24:27], s51 offen sc1\n"
    "v_cvt_pk_bf16_f32 v196, v136, v137\n"
    "v_cvt_pk_bf16_f32 v197, v138, v139\n"
    "v_cvt_pk_bf16_f32 v198, v140, v141\n"
    "v_cvt_pk_bf16_f32 v199, v142, v143\n"
    "buffer_store_dwordx4 v[196:199], v80, s[24:27], s52 offen sc1\n"
    "s_cmp_lt_u32 s62, 13\n"
    "s_cbranch_scc0 .Lgm_nq_33\n"
    "v_mul_f32 v144, s86, v144\n"
    "v_mul_f32 v145, s86, v145\n"
    "v_mul_f32 v146, s86, v146\n"
    "v_mul_f32 v147, s86, v147\n"
    "v_mul_f32 v148, s86, v148\n"
    "v_mul_f32 v149, s86, v149\n"
    "v_mul_f32 v150, s86, v150\n"
    "v_mul_f32 v151, s86, v151\n"
    "v_mul_f32 v152, s86, v152\n"
    "v_mul_f32 v153, s86, v153\n"
    "v_mul_f32 v154, s86, v154\n"
    "v_mul_f32 v155, s86, v155\n"
    "v_mul_f32 v156, s86, v156\n"
    "v_mul_f32 v157, s86, v157\n"
    "v_mul_f32 v158, s86, v158\n"
    "v_mul_f32 v159, s86, v159\n"
    ".Lgm_nq_33:\n"
    "v_cvt_pk_bf16_f32 v200, v144, v145\n"
    "v_cvt_pk_bf16_f32 v201, v146, v147\n"
    "v_cvt_pk_bf16_f32 v202, v148, v149\n"
    "v_cvt_pk_bf16_f32 v203, v150, v151\n"
    "buffer_store_dwordx4 v[200:203], v81, s[24:27], s55 offen sc1\n"
    "v_cvt_pk_bf16_f32 v204, v152, v153\n"
    "v_cvt_pk_bf16_f32 v205, v154, v155\n"
    "v_cvt_pk_bf16_f32 v206, v156, v157\n"
    "v_cvt_pk_bf16_f32 v207, v158, v159\n"
    "buffer_store_dwordx4 v[204:207], v81, s[24:27], s56 offen sc1\n"
    "s_cmp_lt_u32 s62, 12\n"
    "s_cbranch_scc0 .Lgm_nq_34\n"
    "v_mul_f32 v160, s86, v160\n"
    "v_mul_f32 v161, s86, v161\n"
    "v_mul_f32 v162, s86, v162\n"
    "v_mul_f32 v163, s86, v163\n"
    "v_mul_f32 v164, s86, v164\n"
    "v_mul_f32 v165, s86, v165\n"
    "v_mul_f32 v166, s86, v166\n"
    "v_mul_f32 v167, s86, v167\n"
    "v_mul_f32 v168, s86, v168\n"
    "v_mul_f32 v169, s86, v169\n"
    "v_mul_f32 v170, s86, v170\n"
    "v_mul_f32 v171, s86, v171\n"
    "v_mul_f32 v172, s86, v172\n"
    "v_mul_f32 v173, s86, v173\n"
    "v_mul_f32 v174, s86, v174\n"
    "v_mul_f32 v175, s86, v175\n"
    ".Lgm_nq_34:\n"
    "v_cvt_pk_bf16_f32 v208, v160, v161\n"
    "v_cvt_pk_bf16_f32 v209, v162, v163\n"
    "v_cvt_pk_bf16_f32 v210, v164, v165\n"
    "v_cvt_pk_bf16_f32 v211, v166, v167\n"
    "buffer_store_dwordx4 v[208:211], v82, s[24:27], s57 offen sc1\n"
    "v_cvt_pk_bf16_f32 v212, v168, v169\n"
    "v_cvt_pk_bf16_f32 v213, v170, v171\n"
    "v_cvt_pk_bf16_f32 v214, v172, v173\n"
    "v_cvt_pk_bf16_f32 v215, v174, v175\n"
    "buffer_store_dwordx4 v[212:215], v82, s[24:27], s58 offen sc1\n"
    "s_waitcnt vmcnt(0)\n"
    "s_mov_b64 exec, 1\n"
    "ds_add_rtn_u32 v64, v249, v248 offset:0\n"
    "s_waitcnt lgkmcnt(0)\n"
    "v_readfirstlane_b32 s91, v64\n"
    "s_cmp_eq_u32 s91, 3\n"
    "s_cbranch_scc0 .Lgm_arr_35\n"
    "buffer_store_dwordx2 v[250:251], off, s[24:27], s64 sc1\n"
    ".Lgm_arr_35:\n"
    "s_mov_b64 exec, -1\n"
    ".Lgm_e2_28:\n"
    "s_mov_b32 s61, s91\n"
    "s_mov_b64 exec, 1\n"
    "ds_add_u32 v249, v248 offset:12\n"
    "s_mov_b64 exec, -1\n"
    "v_readfirstlane_b32 s74, v250\n"
    "v_readfirstlane_b32 s75, v251\n"
    "v_mov_b32 v128, 0\n"
    "v_mov_b32 v129, 0\n"
    "v_mov_b32 v130, 0\n"
    "v_mov_b32 v131, 0\n"
    "v_mov_b32 v132, 0\n"
    "v_mov_b32 v133, 0\n"
    "v_mov_b32 v134, 0\n"
    "v_mov_b32 v135, 0\n"
    "v_mov_b32 v136, 0\n"
    "v_mov_b32 v137, 0\n"
    "v_mov_b32 v138, 0\n"
    "v_mov_b32 v139, 0\n"
    "v_mov_b32 v140, 0\n"
    "v_mov_b32 v141, 0\n"
    "v_mov_b32 v142, 0\n"
    "v_mov_b32 v143, 0\n"
    "v_mov_b32 v144, 0\n"
    "v_mov_b32 v145, 0\n"
    "v_mov_b32 v146, 0\n"
    "v_mov_b32 v147, 0\n"
    "v_mov_b32 v148, 0\n"
    "v_mov_b32 v149, 0\n"
    "v_mov_b32 v150, 0\n"
    "v_mov_b32 v151, 0\n"
    "v_mov_b32 v152, 0\n"
    "v_mov_b32 v153, 0\n"
    "v_mov_b32 v154, 0\n"
    "v_mov_b32 v155, 0\n"
    "v_mov_b32 v156, 0\n"
    "v_mov_b32 v157, 0\n"
    "v_mov_b32 v158, 0\n"
    "v_mov_b32 v159, 0\n"
    "v_mov_b32 v160, 0\n"
    "v_mov_b32 v161, 0\n"
    "v_mov_b32 v162, 0\n"
    "v_mov_b32 v163, 0\n"
    "v_mov_b32 v164, 0\n"
    "v_mov_b32 v165, 0\n"
    "v_mov_b32 v166, 0\n"
    "v_mov_b32 v167, 0\n"
    "v_mov_b32 v168, 0\n"
    "v_mov_b32 v169, 0\n"
    "v_mov_b32 v170, 0\n"
    "v_mov_b32 v171, 0\n"
    "v_mov_b32 v172, 0\n"
    "v_mov_b32 v173, 0\n"
    "v_mov_b32 v174, 0\n"
    "v_mov_b32 v175, 0\n"
    "v_mov_b32 v176, 0\n"
    "v_mov_b32 v177, 0\n"
    "v_mov_b32 v178, 0\n"
    "v_mov_b32 v179, 0\n"
    "v_mov_b32 v180, 0\n"
    "v_mov_b32 v181, 0\n"
    "v_mov_b32 v182, 0\n"
    "v_mov_b32 v183, 0\n"
    "v_mov_b32 v184, 0\n"
    "v_mov_b32 v185, 0\n"
    "v_mov_b32 v186, 0\n"
    "v_mov_b32 v187, 0\n"
    "v_mov_b32 v188, 0\n"
    "v_mov_b32 v189, 0\n"
    "v_mov_b32 v190, 0\n"
    "v_mov_b32 v191, 0\n"
    "v_mov_b32 v8, v125\n"
    "v_mov_b32 v9, v125\n"
    "v_mov_b32 v10, v125\n"
    "v_mov_b32 v11, v125\n"
    "v_mov_b32 v12, v125\n"
    "v_mov_b32 v13, v125\n"
    "v_mov_b32 v14, v125\n"
    "v_mov_b32 v15, v125\n"
    "v_mov_b32 v24, v125\n"
    "v_mov_b32 v25, v125\n"
    "v_mov_b32 v26, v125\n"
    "v_mov_b32 v27, v125\n"
    "v_mov_b32 v28, v125\n"
    "v_mov_b32 v29, v125\n"
    "v_mov_b32 v30, v125\n"
    "v_mov_b32 v31, v125\n"
    "v_mov_b32 v40, v125\n"
    "v_mov_b32 v41, v125\n"
    "v_mov_b32 v42, v125\n"
    "v_mov_b32 v43, v125\n"
    "v_mov_b32 v44, v125\n"
    "v_mov_b32 v45, v125\n"
    "v_mov_b32 v46, v125\n"
    "v_mov_b32 v47, v125\n"
    "v_mov_b32 v56, v125\n"
    "v_mov_b32 v57, v125\n"
    "v_mov_b32 v58, v125\n"
    "v_mov_b32 v59, v125\n"
    "v_mov_b32 v60, v125\n"
    "v_mov_b32 v61, v125\n"
    "v_mov_b32 v62, v125\n"
    "v_mov_b32 v63, v125\n"
    "s_add_u32 s84, s44, 1\n"
    "s_and_b32 s84, s84, 3\n"
    "s_mul_i32 s84, s84, 24576\n"
    "s_add_u32 s84, s84, 57344\n"
    "v_add_u32 v112, s84, v252\n"
    "s_add_u32 s84, s44, 2\n"
    "s_and_b32 s84, s84, 3\n"
    "s_mul_i32 s84, s84, 24576\n"
    "s_add_u32 s84, s84, 57344\n"
    "v_add_u32 v113, s84, v252\n"
    "s_add_u32 s84, s44, 3\n"
    "s_and_b32 s84, s84, 3\n"
    "s_mul_i32 s84, s84, 24576\n"
    "s_add_u32 s84, s84, 57344\n"
    "v_add_u32 v114, s84, v252\n"
    "s_add_u32 s84, s44, 4\n"
    "s_and_b32 s84, s84, 3\n"
    "s_mul_i32 s84, s84, 24576\n"
    "s_add_u32 s84, s84, 57344\n"
    "v_add_u32 v115, s84, v252\n"
    "v_sub_u32 v116, v237, v248\n"
    "s_lshl_b32 s84, s66, 2\n"
    "s_add_u32 s84, s84, s44\n"
    "s_lshl_b32 s84, s84, 4\n"
    "s_add_u32 s84, s84, s65\n"
    "s_lshl_b32 s84, s84, 11\n"
    "s_add_u32 s51, s84, 13107200\n"
    "s_lshl_b32 s64, s66, 4\n"
    "s_add_u32 s64, s64, s65\n"
    "s_lshl_b32 s64, s64, 7\n"
    "s_add_u32 s64, s64, 65664\n"
    "s_waitcnt vmcnt(0)\n"
    "s_bfm_b64 exec, 18, 0\n"
    "s_mov_b32 s91, 65536\n"
    ".Lgm_poll_36:\n"
    "buffer_load_dwordx2 v[64:65], v124, s[24:27], 0 offen sc1\n"
    "s_waitcnt vmcnt(0)\n"
    "v_cmp_ne_u64 vcc, s[74:75], v[64:65]\n"
    "s_and_b64 vcc, exec, vcc\n"
    "s_cbranch_scc0 .Lgm_polld_37\n"
    "s_sleep 1\n"
    "s_sub_u32 s91, s91, 1\n"
    "s_cbranch_scc0 .Lgm_poll_36\n"
    ".Lgm_polld_37:\n"
    "s_mov_b64 exec, -1\n"
    "buffer_load_dwordx4 v[192:195], v105, s[24:27], s96 offen offset:0 sc1\n"
    "buffer_load_dwordx4 v[196:199], v105, s[24:27], s96 offen offset:1024 sc1\n"
    "buffer_load_dwordx4 v[0:3], v106, s[76:79], s71 offen offset:0 sc1\n"
    "buffer_load_dwordx4 v[4:7], v106, s[76:79], s71 offen offset:1024 sc1\n"
    "buffer_load_dwordx4 v[200:203], v105, s[24:27], s97 offen offset:0 sc1\n"
    "buffer_load_dwordx4 v[204:207], v105, s[24:27], s97 offen offset:1024 sc1\n"
    "s_mov_b64 exec, s[80:81]\n"
    "buffer_load_dwordx4 v[8:11], v107, s[76:79], s71 offen sc1\n"
    "buffer_load_dwordx4 v[12:15], v108, s[76:79], s71 offen sc1\n"
    "s_mov_b64 exec, -1\n"
    "s_add_u32 s71, s71, 86016\n"
    "buffer_load_dwordx4 v[208:211], v105, s[24:27], s98 offen offset:0 sc1\n"
    "buffer_load_dwordx4 v[212:215], v105, s[24:27], s98 offen offset:1024 sc1\n"
    "buffer_load_dwordx4 v[216:219], v105, s[24:27], s99 offen offset:0 sc1\n"
    "buffer_load_dwordx4 v[220:223], v105, s[24:27], s99 offen offset:1024 sc1\n"
    "s_setprio 0\n"
    "s_mov_b32 s70, 2\n"
    "s_waitcnt vmcnt(8)\n"
    "v_and_b32 v192, v109, v192\n"
    "v_and_b32 v193, v109, v193\n"
    "v_and_b32 v198, v110, v198\n"
    "v_and_b32 v199, v110, v199\n"
    "v_mfma_f32_32x32x16_bf16 v[64:79], v[0:3], v[192:195], 0\n"
    "s_nop 0\n"
    "v_mfma_f32_32x32x16_bf16 v[64:79], v[4:7], v[196:199], v[64:79]\n"
    "buffer_load_dwordx4 v[16:19], v106, s[76:79], s71 offen offset:0 sc1\n"
    "buffer_load_dwordx4 v[20:23], v106, s[76:79], s71 offen offset:1024 sc1\n"
    "s_mov_b64 exec, s[80:81]\n"
    "buffer_load_dwordx4 v[24:27], v107, s[76:79], s71 offen sc1\n"
    "buffer_load_dwordx4 v[28:31], v108, s[76:79], s71 offen sc1\n"
    "s_mov_b64 exec, -1\n"
    "s_add_u32 s71, s71, 86016\n"
    "s_waitcnt vmcnt(10)\n"
    "v_and_b32 v200, v109, v200\n"
    "v_and_b32 v201, v109, v201\n"
    "v_and_b32 v206, v110, v206\n"
    "v_and_b32 v207, v110, v207\n"
    "v_mfma_f32_32x32x16_bf16 v[80:95], v[0:3], v[200:203], 0\n"
    "v_exp_f32 v64, v64\n"
    "v_exp_f32 v65, v65\n"
    "v_exp_f32 v66, v66\n"
    "v_exp_f32 v67, v67\n"
    "v_mfma_f32_32x32x16_bf16 v[80:95], v[4:7], v[204:207], v[80:95]\n"
    "v_exp_f32 v68, v68\n"
    "v_exp_f32 v69, v69\n"
    "v_exp_f32 v70, v70\n"
    "v_exp_f32 v71, v71\n"
    "v_exp_f32 v96, v72\n"
    "v_exp_f32 v97, v73\n"
    "v_exp_f32 v98, v74\n"
    "v_exp_f32 v99, v75\n"
    "s_waitcnt vmcnt(8)\n"
    "v_smfmac_f32_32x32x32_bf16 v[128:143], v[8:11], v[64:71], v104\n"
    "v_exp_f32 v100, v76\n"
    "v_exp_f32 v101, v77\n"
    "v_exp_f32 v102, v78\n"
    "v_exp_f32 v103, v79\n"
    "s_waitcnt vmcnt(6)\n"
    "v_and_b32 v208, v109, v208\n"
    "v_and_b32 v209, v109, v209\n"
    "v_and_b32 v214, v110, v214\n"
    "v_and_b32 v215, v110, v215\n"
    "v_mfma_f32_32x32x16_bf16 v[64:79], v[0:3], v[208:211], 0\n"
    "buffer_load_dwordx4 v[32:35], v106, s[76:79], s71 offen offset:0 sc1\n"
    "buffer_load_dwordx4 v[36:39], v106, s[76:79], s71 offen offset:1024 sc1\n"
    "s_mov_b64 exec, s[80:81]\n"
    "buffer_load_dwordx4 v[40:43], v107, s[76:79], s71 offen sc1\n"
    "buffer_load_dwordx4 v[44:47], v108, s[76:79], s71 offen sc1\n"
    "s_mov_b64 exec, -1\n"
    "s_add_u32 s71, s71, 86016\n"
    "v_exp_f32 v80, v80\n"
    "v_exp_f32 v81, v81\n"
    "v_exp_f32 v82, v82\n"
    "v_exp_f32 v83, v83\n"
    "v_mfma_f32_32x32x16_bf16 v[64:79], v[4:7], v[212:215], v[64:79]\n"
    "v_exp_f32 v84, v84\n"
    "v_exp_f32 v85, v85\n"
    "v_exp_f32 v86, v86\n"
    "v_exp_f32 v87, v87\n"
    "v_smfmac_f32_32x32x32_bf16 v[128:143], v[12:15], v[96:103], v104\n"
    "v_exp_f32 v96, v88\n"
    "v_exp_f32 v97, v89\n"
    "v_exp_f32 v98, v90\n"
    "v_exp_f32 v99, v91\n"
    "v_smfmac_f32_32x32x32_bf16 v[144:159], v[8:11], v[80:87], v104\n"
    "v_exp_f32 v100, v92\n"
    "v_exp_f32 v101, v93\n"
    "v_exp_f32 v102, v94\n"
    "v_exp_f32 v103, v95\n"
    "s_waitcnt vmcnt(8)\n"
    "v_and_b32 v216, v109, v216\n"
    "v_and_b32 v217, v109, v217\n"
    "v_and_b32 v222, v110, v222\n"
    "v_and_b32 v223, v110, v223\n"
    "v_mfma_f32_32x32x16_bf16 v[80:95], v[0:3], v[216:219], 0\n"
    "v_exp_f32 v64, v64\n"
    "v_exp_f32 v65, v65\n"
    "v_exp_f32 v66, v66\n"
    "v_exp_f32 v67, v67\n"
    "v_mfma_f32_32x32x16_bf16 v[80:95], v[4:7], v[220:223], v[80:95]\n"
    "v_exp_f32 v68, v68\n"
    "v_exp_f32 v69, v69\n"
    "v_exp_f32 v70, v70\n"
    "v_exp_f32 v71, v71\n"
    "v_smfmac_f32_32x32x32_bf16 v[144:159], v[12:15], v[96:103], v104\n"
    "v_exp_f32 v96, v72\n"
    "v_exp_f32 v97, v73\n"
    "v_exp_f32 v98, v74\n"
    "v_exp_f32 v99, v75\n"
    "v_smfmac_f32_32x32x32_bf16 v[160:175], v[8:11], v[64:71], v104\n"
    "v_exp_f32 v100, v76\n"
    "v_exp_f32 v101, v77\n"
    "v_exp_f32 v102, v78\n"
    "v_exp_f32 v103, v79\n"
    "s_waitcnt vmcnt(4)\n"
    "v_mfma_f32_32x32x16_bf16 v[64:79], v[16:19], v[192:195], 0\n"
    "buffer_load_dwordx4 v[48:51], v106, s[76:79], s71 offen offset:0 sc1\n"
    "buffer_load_dwordx4 v[52:55], v106, s[76:79], s71 offen offset:1024 sc1\n"
    "s_mov_b64 exec, s[80:81]\n"
    "buffer_load_dwordx4 v[56:59], v107, s[76:79], s71 offen sc1\n"
    "buffer_load_dwordx4 v[60:63], v108, s[76:79], s71 offen sc1\n"
    "s_mov_b64 exec, -1\n"
    "s_add_u32 s71, s71, 86016\n"
    "s_cmp_eq_u32 s71, s78\n"
    "s_cselect_b32 s71, s54, s71\n"
    "v_exp_f32 v80, v80\n"
    "v_exp_f32 v81, v81\n"
    "v_exp_f32 v82, v82\n"
    "v_exp_f32 v83, v83\n"
    "v_mfma_f32_32x32x16_bf16 v[64:79], v[20:23], v[196:199], v[64:79]\n"
    "v_exp_f32 v84, v84\n"
    "v_exp_f32 v85, v85\n"
    "v_exp_f32 v86, v86\n"
    "v_exp_f32 v87, v87\n"
    "v_smfmac_f32_32x32x32_bf16 v[160:175], v[12:15], v[96:103], v104\n"
    "v_exp_f32 v96, v88\n"
    "v_exp_f32 v97, v89\n"
    "v_exp_f32 v98, v90\n"
    "v_exp_f32 v99, v91\n"
    "v_smfmac_f32_32x32x32_bf16 v[176:191], v[8:11], v[80:87], v104\n"
    "v_exp_f32 v100, v92\n"
    "v_exp_f32 v101, v93\n"
    "v_exp_f32 v102, v94\n"
    "v_exp_f32 v103, v95\n"
    "v_mfma_f32_32x32x16_bf16 v[80:95], v[16:19], v[200:203], 0\n"
    "s_mov_b64 exec, 1\n"
    "ds_add_u32 v249, v248 offset:44\n"
    "s_mov_b64 exec, -1\n"
    "v_exp_f32 v64, v64\n"
    "v_exp_f32 v65, v65\n"
    "v_exp_f32 v66, v66\n"
    "v_exp_f32 v67, v67\n"
    "v_mfma_f32_32x32x16_bf16 v[80:95], v[20:23], v[204:207], v[80:95]\n"
    "v_exp_f32 v68, v68\n"
    "v_exp_f32 v69, v69\n"
    "v_exp_f32 v70, v70\n"
    "v_exp_f32 v71, v71\n"
    "v_smfmac_f32_32x32x32_bf16 v[176:191], v[12:15], v[96:103], v104\n"
    "v_exp_f32 v96, v72\n"
    "v_exp_f32 v97, v73\n"
    "v_exp_f32 v98, v74\n"
    "v_exp_f32 v99, v75\n"
    "v_smfmac_f32_32x32x32_bf16 v[128:143], v[24:27], v[64:71], v104\n"
    "v_exp_f32 v100, v76\n"
    "v_exp_f32 v101, v77\n"
    "v_exp_f32 v102, v78\n"
    "v_exp_f32 v103, v79\n"
    "v_mfma_f32_32x32x16_bf16 v[64:79], v[16:19], v[208:211], 0\n"
    "buffer_load_dwordx4 v[0:3], v106, s[76:79], s71 offen offset:0 sc1\n"
    "v_exp_f32 v80, v80\n"
    "v_exp_f32 v81, v81\n"
    "v_exp_f32 v82, v82\n"
    "v_exp_f32 v83, v83\n"
    "v_mfma_f32_32x32x16_bf16 v[64:79], v[20:23], v[212:215], v[64:79]\n"
    "buffer_load_dwordx4 v[4:7], v106, s[76:79], s71 offen offset:1024 sc1\n"
    "v_exp_f32 v84, v84\n"
    "v_exp_f32 v85, v85\n"
    "v_exp_f32 v86, v86\n"
    "v_exp_f32 v87, v87\n"
    "v_smfmac_f32_32x32x32_bf16 v[128:143], v[28:31], v[96:103], v104\n"
    "s_mov_b64 exec, s[80:81]\n"
    "buffer_load_dwordx4 v[8:11], v107, s[76:79], s71 offen sc1\n"
    "buffer_load_dwordx4 v[12:15], v108, s[76:79], s71 offen sc1\n"
    "s_mov_b64 exec, -1\n"
    "v_exp_f32 v96, v88\n"
    "v_exp_f32 v97, v89\n"
    "v_exp_f32 v98, v90\n"
    "v_exp_f32 v99, v91\n"
    "v_smfmac_f32_32x32x32_bf16 v[144:159], v[24:27], v[80:87], v104\n"
    "s_add_u32 s71, s71, 86016\n"
    "v_exp_f32 v100, v92\n"
    "v_exp_f32 v101, v93\n"
    "v_exp_f32 v102, v94\n"
    "v_exp_f32 v103, v95\n"
    "v_mfma_f32_32x32x16_bf16 v[80:95], v[16:19], v[216:219], 0\n"
    "v_exp_f32 v64, v64\n"
    "v_exp_f32 v65, v65\n"
    "v_exp_f32 v66, v66\n"
    "v_exp_f32 v67, v67\n"
    "v_mfma_f32_32x32x16_bf16 v[80:95], v[20:23], v[220:223], v[80:95]\n"
    "v_exp_f32 v68, v68\n"
    "v_exp_f32 v69, v69\n"
    "v_exp_f32 v70, v70\n"
    "v_exp_f32 v71, v71\n"
    "v_smfmac_f32_32x32x32_bf16 v[144:159], v[28:31], v[96:103], v104\n"
    "v_exp_f32 v96, v72\n"
    "v_exp_f32 v97, v73\n"
    "v_exp_f32 v98, v74\n"
    "v_exp_f32 v99, v75\n"
    "v_smfmac_f32_32x32x32_bf16 v[160:175], v[24:27], v[64:71], v104\n"
    "s_waitcnt vmcnt(8)\n"
    "v_exp_f32 v100, v76\n"
    "v_exp_f32 v101, v77\n"
    "v_exp_f32 v102, v78\n"
    "v_exp_f32 v103, v79\n"
    "v_mfma_f32_32x32x16_bf16 v[64:79], v[32:35], v[192:195], 0\n"
    "v_exp_f32 v80, v80\n"
    "v_exp_f32 v81, v81\n"
    "v_exp_f32 v82, v82\n"
    "v_exp_f32 v83, v83\n"
    "v_mfma_f32_32x32x16_bf16 v[64:79], v[36:39], v[196:199], v[64:79]\n"
    "v_exp_f32 v84, v84\n"
    "v_exp_f32 v85, v85\n"
    "v_exp_f32 v86, v86\n"
    "v_exp_f32 v87, v87\n"
    "v_smfmac_f32_32x32x32_bf16 v[160:175], v[28:31], v[96:103], v104\n"
    "v_exp_f32 v96, v88\n"
    "v_exp_f32 v97, v89\n"
    "v_exp_f32 v98, v90\n"
    "v_exp_f32 v99, v91\n"
    "v_smfmac_f32_32x32x32_bf16 v[176:191], v[24:27], v[80:87], v104\n"
    "v_exp_f32 v100, v92\n"
    "v_exp_f32 v101, v93\n"
    "v_exp_f32 v102, v94\n"
    "v_exp_f32 v103, v95\n"
    "v_mfma_f32_32x32x16_bf16 v[80:95], v[32:35], v[200:203], 0\n"
    "v_exp_f32 v64, v64\n"
    "v_exp_f32 v65, v65\n"
    "v_exp_f32 v66, v66\n"
    "v_exp_f32 v67, v67\n"
    "v_mfma_f32_32x32x16_bf16 v[80:95], v[36:39], v[204:207], v[80:95]\n"
    "v_exp_f32 v68, v68\n"
    "v_exp_f32 v69, v69\n"
    "v_exp_f32 v70, v70\n"
    "v_exp_f32 v71, v71\n"
    "v_smfmac_f32_32x32x32_bf16 v[176:191], v[28:31], v[96:103], v104\n"
    "v_exp_f32 v96, v72\n"
    "v_exp_f32 v97, v73\n"
    "v_exp_f32 v98, v74\n"
    "v_exp_f32 v99, v75\n"
    "v_smfmac_f32_32x32x32_bf16 v[128:143], v[40:43], v[64:71], v104\n"
    "v_exp_f32 v100, v76\n"
    "v_exp_f32 v101, v77\n"
    "v_exp_f32 v102, v78\n"
    "v_exp_f32 v103, v79\n"
    "v_mfma_f32_32x32x16_bf16 v[64:79], v[32:35], v[208:211], 0\n"
    "buffer_load_dwordx4 v[16:19], v106, s[76:79], s71 offen offset:0 sc1\n"
    "v_exp_f32 v80, v80\n"
    "v_exp_f32 v81, v81\n"
    "v_exp_f32 v82, v82\n"
    "v_exp_f32 v83, v83\n"
    "v_mfma_f32_32x32x16_bf16 v[64:79], v[36:39], v[212:215], v[64:79]\n"
    "buffer_load_dwordx4 v[20:23], v106, s[76:79], s71 offen offset:1024 sc1\n"
    "v_exp_f32 v84, v84\n"
    "v_exp_f32 v85, v85\n"
    "v_exp_f32 v86, v86\n"
    "v_exp_f32 v87, v87\n"
    "v_smfmac_f32_32x32x32_bf16 v[128:143], v[44:47], v[96:103], v104\n"
    "s_mov_b64 exec, s[80:81]\n"
    "buffer_load_dwordx4 v[24:27], v107, s[76:79], s71 offen sc1\n"
    "buffer_load_dwordx4 v[28:31], v108, s[76:79], s71 offen sc1\n"
    "s_mov_b64 exec, -1\n"
    "v_exp_f32 v96, v88\n"
    "v_exp_f32 v97, v89\n"
    "v_exp_f32 v98, v90\n"
    "v_exp_f32 v99, v91\n"
    "v_smfmac_f32_32x32x32_bf16 v[144:159], v[40:43], v[80:87], v104\n"
    "s_add_u32 s71, s71, 86016\n"
    "v_exp_f32 v100, v92\n"
    "v_exp_f32 v101, v93\n"
    "v_exp_f32 v102, v94\n"
    "v_exp_f32 v103, v95\n"
    "v_mfma_f32_32x32x16_bf16 v[80:95], v[32:35], v[216:219], 0\n"
    "v_exp_f32 v64, v64\n"
    "v_exp_f32 v65, v65\n"
    "v_exp_f32 v66, v66\n"
    "v_exp_f32 v67, v67\n"
    "v_mfma_f32_32x32x16_bf16 v[80:95], v[36:39], v[220:223], v[80:95]\n"
    "v_exp_f32 v68, v68\n"
    "v_exp_f32 v69, v69\n"
    "v_exp_f32 v70, v70\n"
    "v_exp_f32 v71, v71\n"
    "v_smfmac_f32_32x32x32_bf16 v[144:159], v[44:47], v[96:103], v104\n"
    "v_exp_f32 v96, v72\n"
    "v_exp_f32 v97, v73\n"
    "v_exp_f32 v98, v74\n"
    "v_exp_f32 v99, v75\n"
    "v_smfmac_f32_32x32x32_bf16 v[160:175], v[40:43], v[64:71], v104\n"
    "s_waitcnt vmcnt(8)\n"
    "v_exp_f32 v100, v76\n"
    "v_exp_f32 v101, v77\n"
    "v_exp_f32 v102, v78\n"
    "v_exp_f32 v103, v79\n"
    "v_mfma_f32_32x32x16_bf16 v[64:79], v[48:51], v[192:195], 0\n"
    "v_exp_f32 v80, v80\n"
    "v_exp_f32 v81, v81\n"
    "v_exp_f32 v82, v82\n"
    "v_exp_f32 v83, v83\n"
    "v_mfma_f32_32x32x16_bf16 v[64:79], v[52:55], v[196:199], v[64:79]\n"
    "v_exp_f32 v84, v84\n"
    "v_exp_f32 v85, v85\n"
    "v_exp_f32 v86, v86\n"
    "v_exp_f32 v87, v87\n"
    "v_smfmac_f32_32x32x32_bf16 v[160:175], v[44:47], v[96:103], v104\n"
    "v_exp_f32 v96, v88\n"
    "v_exp_f32 v97, v89\n"
    "v_exp_f32 v98, v90\n"
    "v_exp_f32 v99, v91\n"
    "v_smfmac_f32_32x32x32_bf16 v[176:191], v[40:43], v[80:87], v104\n"
    "v_exp_f32 v100, v92\n"
    "v_exp_f32 v101, v93\n"
    "v_exp_f32 v102, v94\n"
    "v_exp_f32 v103, v95\n"
    "v_mfma_f32_32x32x16_bf16 v[80:95], v[48:51], v[200:203], 0\n"
    "v_exp_f32 v64, v64\n"
    "v_exp_f32 v65, v65\n"
    "v_exp_f32 v66, v66\n"
    "v_exp_f32 v67, v67\n"
    "v_mfma_f32_32x32x16_bf16 v[80:95], v[52:55], v[204:207], v[80:95]\n"
    "v_exp_f32 v68, v68\n"
    "v_exp_f32 v69, v69\n"
    "v_exp_f32 v70, v70\n"
    "v_exp_f32 v71, v71\n"
    "v_smfmac_f32_32x32x32_bf16 v[176:191], v[44:47], v[96:103], v104\n"
    "v_exp_f32 v96, v72\n"
    "v_exp_f32 v97, v73\n"
    "v_exp_f32 v98, v74\n"
    "v_exp_f32 v99, v75\n"
    "v_smfmac_f32_32x32x32_bf16 v[128:143], v[56:59], v[64:71], v104\n"
    "v_exp_f32 v100, v76\n"
    "v_exp_f32 v101, v77\n"
    "v_exp_f32 v102, v78\n"
    "v_exp_f32 v103, v79\n"
    "v_mfma_f32_32x32x16_bf16 v[64:79], v[48:51], v[208:211], 0\n"
    "buffer_load_dwordx4 v[32:35], v106, s[76:79], s71 offen offset:0 sc1\n"
    "v_exp_f32 v80, v80\n"
    "v_exp_f32 v81, v81\n"
    "v_exp_f32 v82, v82\n"
    "v_exp_f32 v83, v83\n"
    "v_mfma_f32_32x32x16_bf16 v[64:79], v[52:55], v[212:215], v[64:79]\n"
    "buffer_load_dwordx4 v[36:39], v106, s[76:79], s71 offen offset:1024 sc1\n"
    "v_exp_f32 v84, v84\n"
    "v_exp_f32 v85, v85\n"
    "v_exp_f32 v86, v86\n"
    "v_exp_f32 v87, v87\n"
    "v_smfmac_f32_32x32x32_bf16 v[128:143], v[60:63], v[96:103], v104\n"
    "s_mov_b64 exec, s[80:81]\n"
    "buffer_load_dwordx4 v[40:43], v107, s[76:79], s71 offen sc1\n"
    "buffer_load_dwordx4 v[44:47], v108, s[76:79], s71 offen sc1\n"
    "s_mov_b64 exec, -1\n"
    "v_exp_f32 v96, v88\n"
    "v_exp_f32 v97, v89\n"
    "v_exp_f32 v98, v90\n"
    "v_exp_f32 v99, v91\n"
    "v_smfmac_f32_32x32x32_bf16 v[144:159], v[56:59], v[80:87], v104\n"
    "s_add_u32 s71, s71, 86016\n"
    "v_exp_f32 v100, v92\n"
    "v_exp_f32 v101, v93\n"
    "v_exp_f32 v102, v94\n"
    "v_exp_f32 v103, v95\n"
    "v_mfma_f32_32x32x16_bf16 v[80:95], v[48:51], v[216:219], 0\n"
    "v_exp_f32 v64, v64\n"
    "v_exp_f32 v65, v65\n"
    "v_exp_f32 v66, v66\n"
    "v_exp_f32 v67, v67\n"
    "v_mfma_f32_32x32x16_bf16 v[80:95], v[52:55], v[220:223], v[80:95]\n"
    "v_exp_f32 v68, v68\n"
    "v_exp_f32 v69, v69\n"
    "v_exp_f32 v70, v70\n"
    "v_exp_f32 v71, v71\n"
    "v_smfmac_f32_32x32x32_bf16 v[144:159], v[60:63], v[96:103], v104\n"
    "v_exp_f32 v96, v72\n"
    "v_exp_f32 v97, v73\n"
    "v_exp_f32 v98, v74\n"
    "v_exp_f32 v99, v75\n"
    "v_smfmac_f32_32x32x32_bf16 v[160:175], v[56:59], v[64:71], v104\n"
    "s_waitcnt vmcnt(8)\n"
    "v_exp_f32 v100, v76\n"
    "v_exp_f32 v101, v77\n"
    "v_exp_f32 v102, v78\n"
    "v_exp_f32 v103, v79\n"
    "v_mfma_f32_32x32x16_bf16 v[64:79], v[0:3], v[192:195], 0\n"
    "v_exp_f32 v80, v80\n"
    "v_exp_f32 v81, v81\n"
    "v_exp_f32 v82, v82\n"
    "v_exp_f32 v83, v83\n"
    "v_mfma_f32_32x32x16_bf16 v[64:79], v[4:7], v[196:199], v[64:79]\n"
    "v_exp_f32 v84, v84\n"
    "v_exp_f32 v85, v85\n"
    "v_exp_f32 v86, v86\n"
    "v_exp_f32 v87, v87\n"
    "v_smfmac_f32_32x32x32_bf16 v[160:175], v[60:63], v[96:103], v104\n"
    "v_exp_f32 v96, v88\n"
    "v_exp_f32 v97, v89\n"
    "v_exp_f32 v98, v90\n"
    "v_exp_f32 v99, v91\n"
    "v_smfmac_f32_32x32x32_bf16 v[176:191], v[56:59], v[80:87], v104\n"
    "v_exp_f32 v100, v92\n"
    "v_exp_f32 v101, v93\n"
    "v_exp_f32 v102, v94\n"
    "v_exp_f32 v103, v95\n"
    ".Lgm_att_38:\n"
    "v_mfma_f32_32x32x16_bf16 v[80:95], v[0:3], v[200:203], 0\n"
    "v_exp_f32 v64, v64\n"
    "v_exp_f32 v65, v65\n"
    "v_exp_f32 v66, v66\n"
    "v_exp_f32 v67, v67\n"
    "v_mfma_f32_32x32x16_bf16 v[80:95], v[4:7], v[204:207], v[80:95]\n"
    "v_exp_f32 v68, v68\n"
    "v_exp_f32 v69, v69\n"
    "v_exp_f32 v70, v70\n"
    "v_exp_f32 v71, v71\n"
    "v_smfmac_f32_32x32x32_bf16 v[176:191], v[60:63], v[96:103], v104\n"
    "v_exp_f32 v96, v72\n"
    "v_exp_f32 v97, v73\n"
    "v_exp_f32 v98, v74\n"
    "v_exp_f32 v99, v75\n"
    "v_smfmac_f32_32x32x32_bf16 v[128:143], v[8:11], v[64:71], v104\n"
    "v_exp_f32 v100, v76\n"
    "v_exp_f32 v101, v77\n"
    "v_exp_f32 v102, v78\n"
    "v_exp_f32 v103, v79\n"
    "v_mfma_f32_32x32x16_bf16 v[64:79], v[0:3], v[208:211], 0\n"
    "buffer_load_dwordx4 v[48:51], v106, s[76:79], s71 offen offset:0 sc1\n"
    "v_exp_f32 v80, v80\n"
    "v_exp_f32 v81, v81\n"
    "v_exp_f32 v82, v82\n"
    "v_exp_f32 v83, v83\n"
    "v_mfma_f32_32x32x16_bf16 v[64:79], v[4:7], v[212:215], v[64:79]\n"
    "buffer_load_dwordx4 v[52:55], v106, s[76:79], s71 offen offset:1024 sc1\n"
    "v_exp_f32 v84, v84\n"
    "v_exp_f32 v85, v85\n"
    "v_exp_f32 v86, v86\n"
    "v_exp_f32 v87, v87\n"
    "v_smfmac_f32_32x32x32_bf16 v[128:143], v[12:15], v[96:103], v104\n"
    "s_mov_b64 exec, s[80:81]\n"
    "buffer_load_dwordx4 v[56:59], v107, s[76:79], s71 offen sc1\n"
    "buffer_load_dwordx4 v[60:63], v108, s[76:79], s71 offen sc1\n"
    "s_mov_b64 exec, -1\n"
    "v_exp_f32 v96, v88\n"
    "v_exp_f32 v97, v89\n"
    "v_exp_f32 v98, v90\n"
    "v_exp_f32 v99, v91\n"
    "v_smfmac_f32_32x32x32_bf16 v[144:159], v[8:11], v[80:87], v104\n"
    "s_add_u32 s71, s71, 86016\n"
    "v_exp_f32 v100, v92\n"
    "v_exp_f32 v101, v93\n"
    "v_exp_f32 v102, v94\n"
    "v_exp_f32 v103, v95\n"
    "v_mfma_f32_32x32x16_bf16 v[80:95], v[0:3], v[216:219], 0\n"
    "s_cmp_eq_u32 s71, s78\n"
    "v_exp_f32 v64, v64\n"
    "v_exp_f32 v65, v65\n"
    "v_exp_f32 v66, v66\n"
    "v_exp_f32 v67, v67\n"
    "v_mfma_f32_32x32x16_bf16 v[80:95], v[4:7], v[220:223], v[80:95]\n"
    "s_cselect_b32 s71, s54, s71\n"
    "v_exp_f32 v68, v68\n"
    "v_exp_f32 v69, v69\n"
    "v_exp_f32 v70, v70\n"
    "v_exp_f32 v71, v71\n"
    "v_smfmac_f32_32x32x32_bf16 v[144:159], v[12:15], v[96:103], v104\n"
    "v_exp_f32 v96, v72\n"
    "v_exp_f32 v97, v73\n"
    "v_exp_f32 v98, v74\n"
    "v_exp_f32 v99, v75\n"
    "v_smfmac_f32_32x32x32_bf16 v[160:175], v[8:11], v[64:71], v104\n"
    "s_waitcnt vmcnt(8)\n"
    "v_exp_f32 v100, v76\n"
    "v_exp_f32 v101, v77\n"
    "v_exp_f32 v102, v78\n"
    "v_exp_f32 v103, v79\n"
    "v_mfma_f32_32x32x16_bf16 v[64:79], v[16:19], v[192:195], 0\n"
    "v_exp_f32 v80, v80\n"
    "v_exp_f32 v81, v81\n"
    "v_exp_f32 v82, v82\n"
    "v_exp_f32 v83, v83\n"
    "v_mfma_f32_32x32x16_bf16 v[64:79], v[20:23], v[196:199], v[64:79]\n"
    "v_exp_f32 v84, v84\n"
    "v_exp_f32 v85, v85\n"
    "v_exp_f32 v86, v86\n"
    "v_exp_f32 v87, v87\n"
    "v_smfmac_f32_32x32x32_bf16 v[160:175], v[12:15], v[96:103], v104\n"
    "v_exp_f32 v96, v88\n"
    "v_exp_f32 v97, v89\n"
    "v_exp_f32 v98, v90\n"
    "v_exp_f32 v99, v91\n"
    "v_smfmac_f32_32x32x32_bf16 v[176:191], v[8:11], v[80:87], v104\n"
    "v_exp_f32 v100, v92\n"
    "v_exp_f32 v101, v93\n"
    "v_exp_f32 v102, v94\n"
    "v_exp_f32 v103, v95\n"
    "v_mfma_f32_32x32x16_bf16 v[80:95], v[16:19], v[200:203], 0\n"
    "v_exp_f32 v64, v64\n"
    "v_exp_f32 v65, v65\n"
    "v_exp_f32 v66, v66\n"
    "v_exp_f32 v67, v67\n"
    "v_mfma_f32_32x32x16_bf16 v[80:95], v[20:23], v[204:207], v[80:95]\n"
    "v_exp_f32 v68, v68\n"
    "v_exp_f32 v69, v69\n"
    "v_exp_f32 v70, v70\n"
    "v_exp_f32 v71, v71\n"
    "v_smfmac_f32_32x32x32_bf16 v[176:191], v[12:15], v[96:103], v104\n"
    "v_exp_f32 v96, v72\n"
    "v_exp_f32 v97, v73\n"
    "v_exp_f32 v98, v74\n"
    "v_exp_f32 v99, v75\n"
    "v_smfmac_f32_32x32x32_bf16 v[128:143], v[24:27], v[64:71], v104\n"
    "v_exp_f32 v100, v76\n"
    "v_exp_f32 v101, v77\n"
    "v_exp_f32 v102, v78\n"
    "v_exp_f32 v103, v79\n"
    "v_mfma_f32_32x32x16_bf16 v[64:79], v[16:19], v[208:211], 0\n"
    "buffer_load_dwordx4 v[0:3], v106, s[76:79], s71 offen offset:0 sc1\n"
    "v_exp_f32 v80, v80\n"
    "v_exp_f32 v81, v81\n"
    "v_exp_f32 v82, v82\n"
    "v_exp_f32 v83, v83\n"
    "v_mfma_f32_32x32x16_bf16 v[64:79], v[20:23], v[212:215], v[64:79]\n"
    "buffer_load_dwordx4 v[4:7], v106, s[76:79], s71 offen offset:1024 sc1\n"
    "v_exp_f32 v84, v84\n"
    "v_exp_f32 v85, v85\n"
    "v_exp_f32 v86, v86\n"
    "v_exp_f32 v87, v87\n"
    "v_smfmac_f32_32x32x32_bf16 v[128:143], v[28:31], v[96:103], v104\n"
    "s_mov_b64 exec, s[80:81]\n"
    "buffer_load_dwordx4 v[8:11], v107, s[76:79], s71 offen sc1\n"
    "buffer_load_dwordx4 v[12:15], v108, s[76:79], s71 offen sc1\n"
    "s_mov_b64 exec, -1\n"
    "v_exp_f32 v96, v88\n"
    "v_exp_f32 v97, v89\n"
    "v_exp_f32 v98, v90\n"
    "v_exp_f32 v99, v91\n"
    "v_smfmac_f32_32x32x32_bf16 v[144:159], v[24:27], v[80:87], v104\n"
    "s_add_u32 s71, s71, 86016\n"
    "v_exp_f32 v100, v92\n"
    "v_exp_f32 v101, v93\n"
    "v_exp_f32 v102, v94\n"
    "v_exp_f32 v103, v95\n"
    "v_mfma_f32_32x32x16_bf16 v[80:95], v[16:19], v[216:219], 0\n"
    "v_exp_f32 v64, v64\n"
    "v_exp_f32 v65, v65\n"
    "v_exp_f32 v66, v66\n"
    "v_exp_f32 v67, v67\n"
    "v_mfma_f32_32x32x16_bf16 v[80:95], v[20:23], v[220:223], v[80:95]\n"
    "v_exp_f32 v68, v68\n"
    "v_exp_f32 v69, v69\n"
    "v_exp_f32 v70, v70\n"
    "v_exp_f32 v71, v71\n"
    "v_smfmac_f32_32x32x32_bf16 v[144:159], v[28:31], v[96:103], v104\n"
    "v_exp_f32 v96, v72\n"
    "v_exp_f32 v97, v73\n"
    "v_exp_f32 v98, v74\n"
    "v_exp_f32 v99, v75\n"
    "v_smfmac_f32_32x32x32_bf16 v[160:175], v[24:27], v[64:71], v104\n"
    "s_waitcnt vmcnt(8)\n"
    "v_exp_f32 v100, v76\n"
    "v_exp_f32 v101, v77\n"
    "v_exp_f32 v102, v78\n"
    "v_exp_f32 v103, v79\n"
    "v_mfma_f32_32x32x16_bf16 v[64:79], v[32:35], v[192:195], 0\n"
    "v_exp_f32 v80, v80\n"
    "v_exp_f32 v81, v81\n"
    "v_exp_f32 v82, v82\n"
    "v_exp_f32 v83, v83\n"
    "v_mfma_f32_32x32x16_bf16 v[64:79], v[36:39], v[196:199], v[64:79]\n"
    "v_exp_f32 v84, v84\n"
    "v_exp_f32 v85, v85\n"
    "v_exp_f32 v86, v86\n"
    "v_exp_f32 v87, v87\n"
    "v_smfmac_f32_32x32x32_bf16 v[160:175], v[28:31], v[96:103], v104\n"
    "v_exp_f32 v96, v88\n"
    "v_exp_f32 v97, v89\n"
    "v_exp_f32 v98, v90\n"
    "v_exp_f32 v99, v91\n"
    "v_smfmac_f32_32x32x32_bf16 v[176:191], v[24:27], v[80:87], v104\n"
    "v_exp_f32 v100, v92\n"
    "v_exp_f32 v101, v93\n"
    "v_exp_f32 v102, v94\n"
    "v_exp_f32 v103, v95\n"
    "v_mfma_f32_32x32x16_bf16 v[80:95], v[32:35], v[200:203], 0\n"
    "v_exp_f32 v64, v64\n"
    "v_exp_f32 v65, v65\n"
    "v_exp_f32 v66, v66\n"
    "v_exp_f32 v67, v67\n"
    "v_mfma_f32_32x32x16_bf16 v[80:95], v[36:39], v[204:207], v[80:95]\n"
    "v_exp_f32 v68, v68\n"
    "v_exp_f32 v69, v69\n"
    "v_exp_f32 v70, v70\n"
    "v_exp_f32 v71, v71\n"
    "v_smfmac_f32_32x32x32_bf16 v[176:191], v[28:31], v[96:103], v104\n"
    "v_exp_f32 v96, v72\n"
    "v_exp_f32 v97, v73\n"
    "v_exp_f32 v98, v74\n"
    "v_exp_f32 v99, v75\n"
    "v_smfmac_f32_32x32x32_bf16 v[128:143], v[40:43], v[64:71], v104\n"
    "v_exp_f32 v100, v76\n"
    "v_exp_f32 v101, v77\n"
    "v_exp_f32 v102, v78\n"
    "v_exp_f32 v103, v79\n"
    "v_mfma_f32_32x32x16_bf16 v[64:79], v[32:35], v[208:211], 0\n"
    "buffer_load_dwordx4 v[16:19], v106, s[76:79], s71 offen offset:0 sc1\n"
    "v_exp_f32 v80, v80\n"
    "v_exp_f32 v81, v81\n"
    "v_exp_f32 v82, v82\n"
    "v_exp_f32 v83, v83\n"
    "v_mfma_f32_32x32x16_bf16 v[64:79], v[36:39], v[212:215], v[64:79]\n"
    "buffer_load_dwordx4 v[20:23], v106, s[76:79], s71 offen offset:1024 sc1\n"
    "v_exp_f32 v84, v84\n"
    "v_exp_f32 v85, v85\n"
    "v_exp_f32 v86, v86\n"
    "v_exp_f32 v87, v87\n"
    "v_smfmac_f32_32x32x32_bf16 v[128:143], v[44:47], v[96:103], v104\n"
    "s_mov_b64 exec, s[80:81]\n"
    "buffer_load_dwordx4 v[24:27], v107, s[76:79], s71 offen sc1\n"
    "buffer_load_dwordx4 v[28:31], v108, s[76:79], s71 offen sc1\n"
    "s_mov_b64 exec, -1\n"
    "v_exp_f32 v96, v88\n"
    "v_exp_f32 v97, v89\n"
    "v_exp_f32 v98, v90\n"
    "v_exp_f32 v99, v91\n"
    "v_smfmac_f32_32x32x32_bf16 v[144:159], v[40:43], v[80:87], v104\n"
    "s_add_u32 s71, s71, 86016\n"
    "v_exp_f32 v100, v92\n"
    "v_exp_f32 v101, v93\n"
    "v_exp_f32 v102, v94\n"
    "v_exp_f32 v103, v95\n"
    "v_mfma_f32_32x32x16_bf16 v[80:95], v[32:35], v[216:219], 0\n"
    "v_exp_f32 v64, v64\n"
    "v_exp_f32 v65, v65\n"
    "v_exp_f32 v66, v66\n"
    "v_exp_f32 v67, v67\n"
    "v_mfma_f32_32x32x16_bf16 v[80:95], v[36:39], v[220:223], v[80:95]\n"
    "v_exp_f32 v68, v68\n"
    "v_exp_f32 v69, v69\n"
    "v_exp_f32 v70, v70\n"
    "v_exp_f32 v71, v71\n"
    "v_smfmac_f32_32x32x32_bf16 v[144:159], v[44:47], v[96:103], v104\n"
    "v_exp_f32 v96, v72\n"
    "v_exp_f32 v97, v73\n"
    "v_exp_f32 v98, v74\n"
    "v_exp_f32 v99, v75\n"
    "v_smfmac_f32_32x32x32_bf16 v[160:175], v[40:43], v[64:71], v104\n"
    "s_waitcnt vmcnt(8)\n"
    "v_exp_f32 v100, v76\n"
    "v_exp_f32 v101, v77\n"
    "v_exp_f32 v102, v78\n"
    "v_exp_f32 v103, v79\n"
    "v_mfma_f32_32x32x16_bf16 v[64:79], v[48:51], v[192:195], 0\n"
    "v_exp_f32 v80, v80\n"
    "v_exp_f32 v81, v81\n"
    "v_exp_f32 v82, v82\n"
    "v_exp_f32 v83, v83\n"
    "v_mfma_f32_32x32x16_bf16 v[64:79], v[52:55], v[196:199], v[64:79]\n"
    "v_exp_f32 v84, v84\n"
    "v_exp_f32 v85, v85\n"
    "v_exp_f32 v86, v86\n"
    "v_exp_f32 v87, v87\n"
    "v_smfmac_f32_32x32x32_bf16 v[160:175], v[44:47], v[96:103], v104\n"
    "v_exp_f32 v96, v88\n"
    "v_exp_f32 v97, v89\n"
    "v_exp_f32 v98, v90\n"
    "v_exp_f32 v99, v91\n"
    "v_smfmac_f32_32x32x32_bf16 v[176:191], v[40:43], v[80:87], v104\n"
    "v_exp_f32 v100, v92\n"
    "v_exp_f32 v101, v93\n"
    "v_exp_f32 v102, v94\n"
    "v_exp_f32 v103, v95\n"
    "v_mfma_f32_32x32x16_bf16 v[80:95], v[48:51], v[200:203], 0\n"
    "v_exp_f32 v64, v64\n"
    "v_exp_f32 v65, v65\n"
    "v_exp_f32 v66, v66\n"
    "v_exp_f32 v67, v67\n"
    "v_mfma_f32_32x32x16_bf16 v[80:95], v[52:55], v[204:207], v[80:95]\n"
    "v_exp_f32 v68, v68\n"
    "v_exp_f32 v69, v69\n"
    "v_exp_f32 v70, v70\n"
    "v_exp_f32 v71, v71\n"
    "v_smfmac_f32_32x32x32_bf16 v[176:191], v[44:47], v[96:103], v104\n"
    "v_exp_f32 v96, v72\n"
    "v_exp_f32 v97, v73\n"
    "v_exp_f32 v98, v74\n"
    "v_exp_f32 v99, v75\n"
    "v_smfmac_f32_32x32x32_bf16 v[128:143], v[56:59], v[64:71], v104\n"
    "v_exp_f32 v100, v76\n"
    "v_exp_f32 v101, v77\n"
    "v_exp_f32 v102, v78\n"
    "v_exp_f32 v103, v79\n"
    "v_mfma_f32_32x32x16_bf16 v[64:79], v[48:51], v[208:211], 0\n"
    "buffer_load_dwordx4 v[32:35], v106, s[76:79], s71 offen offset:0 sc1\n"
    "v_exp_f32 v80, v80\n"
    "v_exp_f32 v81, v81\n"
    "v_exp_f32 v82, v82\n"
    "v_exp_f32 v83, v83\n"
    "v_mfma_f32_32x32x16_bf16 v[64:79], v[52:55], v[212:215], v[64:79]\n"
    "buffer_load_dwordx4 v[36:39], v106, s[76:79], s71 offen offset:1024 sc1\n"
    "v_exp_f32 v84, v84\n"
    "v_exp_f32 v85, v85\n"
    "v_exp_f32 v86, v86\n"
    "v_exp_f32 v87, v87\n"
    "v_smfmac_f32_32x32x32_bf16 v[128:143], v[60:63], v[96:103], v104\n"
    "s_mov_b64 exec, s[80:81]\n"
    "buffer_load_dwordx4 v[40:43], v107, s[76:79], s71 offen sc1\n"
    "buffer_load_dwordx4 v[44:47], v108, s[76:79], s71 offen sc1\n"
    "s_mov_b64 exec, -1\n"
    "v_exp_f32 v96, v88\n"
    "v_exp_f32 v97, v89\n"
    "v_exp_f32 v98, v90\n"
    "v_exp_f32 v99, v91\n"
    "v_smfmac_f32_32x32x32_bf16 v[144:159], v[56:59], v[80:87], v104\n"
    "s_add_u32 s71, s71, 86016\n"
    "v_exp_f32 v100, v92\n"
    "v_exp_f32 v101, v93\n"
    "v_exp_f32 v102, v94\n"
    "v_exp_f32 v103, v95\n"
    "v_mfma_f32_32x32x16_bf16 v[80:95], v[48:51], v[216:219], 0\n"
    "v_exp_f32 v64, v64\n"
    "v_exp_f32 v65, v65\n"
    "v_exp_f32 v66, v66\n"
    "v_exp_f32 v67, v67\n"
    "v_mfma_f32_32x32x16_bf16 v[80:95], v[52:55], v[220:223], v[80:95]\n"
    "v_exp_f32 v68, v68\n"
    "v_exp_f32 v69, v69\n"
    "v_exp_f32 v70, v70\n"
    "v_exp_f32 v71, v71\n"
    "v_smfmac_f32_32x32x32_bf16 v[144:159], v[60:63], v[96:103], v104\n"
    "v_exp_f32 v96, v72\n"
    "v_exp_f32 v97, v73\n"
    "v_exp_f32 v98, v74\n"
    "v_exp_f32 v99, v75\n"
    "v_smfmac_f32_32x32x32_bf16 v[160:175], v[56:59], v[64:71], v104\n"
    "s_waitcnt vmcnt(8)\n"
    "v_exp_f32 v100, v76\n"
    "v_exp_f32 v101, v77\n"
    "v_exp_f32 v102, v78\n"
    "v_exp_f32 v103, v79\n"
    "v_mfma_f32_32x32x16_bf16 v[64:79], v[0:3], v[192:195], 0\n"
    "s_sub_u32 s70, s70, 1\n"
    "v_exp_f32 v80, v80\n"
    "v_exp_f32 v81, v81\n"
    "v_exp_f32 v82, v82\n"
    "v_exp_f32 v83, v83\n"
    "v_mfma_f32_32x32x16_bf16 v[64:79], v[4:7], v[196:199], v[64:79]\n"
    "s_cmp_lg_u32 s70, 0\n"
    "v_exp_f32 v84, v84\n"
    "v_exp_f32 v85, v85\n"
    "v_exp_f32 v86, v86\n"
    "v_exp_f32 v87, v87\n"
    "v_smfmac_f32_32x32x32_bf16 v[160:175], v[60:63], v[96:103], v104\n"
    "v_exp_f32 v96, v88\n"
    "v_exp_f32 v97, v89\n"
    "v_exp_f32 v98, v90\n"
    "v_exp_f32 v99, v91\n"
    "v_smfmac_f32_32x32x32_bf16 v[176:191], v[56:59], v[80:87], v104\n"
    "v_exp_f32 v100, v92\n"
    "v_exp_f32 v101, v93\n"
    "v_exp_f32 v102, v94\n"
    "v_exp_f32 v103, v95\n"
    "s_cbranch_scc1 .Lgm_att_38\n"
    "v_mfma_f32_32x32x16_bf16 v[80:95], v[0:3], v[200:203], 0\n"
    "v_exp_f32 v64, v64\n"
    "v_exp_f32 v65, v65\n"
    "v_exp_f32 v66, v66\n"
    "v_exp_f32 v67, v67\n"
    "v_mfma_f32_32x32x16_bf16 v[80:95], v[4:7], v[204:207], v[80:95]\n"
    "v_exp_f32 v68, v68\n"
    "v_exp_f32 v69, v69\n"
    "v_exp_f32 v70, v70\n"
    "v_exp_f32 v71, v71\n"
    "v_smfmac_f32_32x32x32_bf16 v[176:191], v[60:63], v[96:103], v104\n"
    "v_exp_f32 v96, v72\n"
    "v_exp_f32 v97, v73\n"
    "v_exp_f32 v98, v74\n"
    "v_exp_f32 v99, v75\n"
    "v_smfmac_f32_32x32x32_bf16 v[128:143], v[8:11], v[64:71], v104\n"
    "v_exp_f32 v100, v76\n"
    "v_exp_f32 v101, v77\n"
    "v_exp_f32 v102, v78\n"
    "v_exp_f32 v103, v79\n"
    "v_mfma_f32_32x32x16_bf16 v[64:79], v[0:3], v[208:211], 0\n"
    "buffer_load_dwordx4 v[48:51], v106, s[76:79], s71 offen offset:0 sc1\n"
    "v_exp_f32 v80, v80\n"
    "v_exp_f32 v81, v81\n"
    "v_exp_f32 v82, v82\n"
    "v_exp_f32 v83, v83\n"
    "v_mfma_f32_32x32x16_bf16 v[64:79], v[4:7], v[212:215], v[64:79]\n"
    "buffer_load_dwordx4 v[52:55], v106, s[76:79], s71 offen offset:1024 sc1\n"
    "v_exp_f32 v84, v84\n"
    "v_exp_f32 v85, v85\n"
    "v_exp_f32 v86, v86\n"
    "v_exp_f32 v87, v87\n"
    "v_smfmac_f32_32x32x32_bf16 v[128:143], v[12:15], v[96:103], v104\n"
    "s_mov_b64 exec, s[80:81]\n"
    "buffer_load_dwordx4 v[56:59], v107, s[76:79], s71 offen sc1\n"
    "buffer_load_dwordx4 v[60:63], v108, s[76:79], s71 offen sc1\n"
    "s_mov_b64 exec, -1\n"
    "v_exp_f32 v96, v88\n"
    "v_exp_f32 v97, v89\n"
    "v_exp_f32 v98, v90\n"
    "v_exp_f32 v99, v91\n"
    "v_smfmac_f32_32x32x32_bf16 v[144:159], v[8:11], v[80:87], v104\n"
    "s_add_u32 s71, s71, 86016\n"
    "v_exp_f32 v100, v92\n"
    "v_exp_f32 v101, v93\n"
    "v_exp_f32 v102, v94\n"
    "v_exp_f32 v103, v95\n"
    "v_mfma_f32_32x32x16_bf16 v[80:95], v[0:3], v[216:219], 0\n"
    "v_exp_f32 v64, v64\n"
    "v_exp_f32 v65, v65\n"
    "v_exp_f32 v66, v66\n"
    "v_exp_f32 v67, v67\n"
    "v_mfma_f32_32x32x16_bf16 v[80:95], v[4:7], v[220:223], v[80:95]\n"
    "v_exp_f32 v68, v68\n"
    "v_exp_f32 v69, v69\n"
    "v_exp_f32 v70, v70\n"
    "v_exp_f32 v71, v71\n"
    "v_smfmac_f32_32x32x32_bf16 v[144:159], v[12:15], v[96:103], v104\n"
    "v_exp_f32 v96, v72\n"
    "v_exp_f32 v97, v73\n"
    "v_exp_f32 v98, v74\n"
    "v_exp_f32 v99, v75\n"
    "v_smfmac_f32_32x32x32_bf16 v[160:175], v[8:11], v[64:71], v104\n"
    "s_waitcnt vmcnt(8)\n"
    "v_exp_f32 v100, v76\n"
    "v_exp_f32 v101, v77\n"
    "v_exp_f32 v102, v78\n"
    "v_exp_f32 v103, v79\n"
    "v_mfma_f32_32x32x16_bf16 v[64:79], v[16:19], v[192:195], 0\n"
    "v_exp_f32 v80, v80\n"
    "v_exp_f32 v81, v81\n"
    "v_exp_f32 v82, v82\n"
    "v_exp_f32 v83, v83\n"
    "v_mfma_f32_32x32x16_bf16 v[64:79], v[20:23], v[196:199], v[64:79]\n"
    "v_exp_f32 v84, v84\n"
    "v_exp_f32 v85, v85\n"
    "v_exp_f32 v86, v86\n"
    "v_exp_f32 v87, v87\n"
    "v_smfmac_f32_32x32x32_bf16 v[160:175], v[12:15], v[96:103], v104\n"
    "v_exp_f32 v96, v88\n"
    "v_exp_f32 v97, v89\n"
    "v_exp_f32 v98, v90\n"
    "v_exp_f32 v99, v91\n"
    "v_smfmac_f32_32x32x32_bf16 v[176:191], v[8:11], v[80:87], v104\n"
    "v_exp_f32 v100, v92\n"
    "v_exp_f32 v101, v93\n"
    "v_exp_f32 v102, v94\n"
    "v_exp_f32 v103, v95\n"
    "v_mfma_f32_32x32x16_bf16 v[80:95], v[16:19], v[200:203], 0\n"
    "v_exp_f32 v64, v64\n"
    "v_exp_f32 v65, v65\n"
    "v_exp_f32 v66, v66\n"
    "v_exp_f32 v67, v67\n"
    "v_mfma_f32_32x32x16_bf16 v[80:95], v[20:23], v[204:207], v[80:95]\n"
    "v_exp_f32 v68, v68\n"
    "v_exp_f32 v69, v69\n"
    "v_exp_f32 v70, v70\n"
    "v_exp_f32 v71, v71\n"
    "v_smfmac_f32_32x32x32_bf16 v[176:191], v[12:15], v[96:103], v104\n"
    "v_exp_f32 v96, v72\n"
    "v_exp_f32 v97, v73\n"
    "v_exp_f32 v98, v74\n"
    "v_exp_f32 v99, v75\n"
    "v_smfmac_f32_32x32x32_bf16 v[128:143], v[24:27], v[64:71], v104\n"
    "v_exp_f32 v100, v76\n"
    "v_exp_f32 v101, v77\n"
    "v_exp_f32 v102, v78\n"
    "v_exp_f32 v103, v79\n"
    "v_mfma_f32_32x32x16_bf16 v[64:79], v[16:19], v[208:211], 0\n"
    "ds_read_b32 v127, v249 offset:12\n"
    "v_exp_f32 v80, v80\n"
    "v_exp_f32 v81, v81\n"
    "v_exp_f32 v82, v82\n"
    "v_exp_f32 v83, v83\n"
    "v_mfma_f32_32x32x16_bf16 v[64:79], v[20:23], v[212:215], v[64:79]\n"
    "v_exp_f32 v84, v84\n"
    "v_exp_f32 v85, v85\n"
    "v_exp_f32 v86, v86\n"
    "v_exp_f32 v87, v87\n"
    "v_smfmac_f32_32x32x32_bf16 v[128:143], v[28:31], v[96:103], v104\n"
    "v_exp_f32 v96, v88\n"
    "v_exp_f32 v97, v89\n"
    "v_exp_f32 v98, v90\n"
    "v_exp_f32 v99, v91\n"
    "v_smfmac_f32_32x32x32_bf16 v[144:159], v[24:27], v[80:87], v104\n"
    "v_exp_f32 v100, v92\n"
    "v_exp_f32 v101, v93\n"
    "v_exp_f32 v102, v94\n"
    "v_exp_f32 v103, v95\n"
    "v_mfma_f32_32x32x16_bf16 v[80:95], v[16:19], v[216:219], 0\n"
    "v_exp_f32 v64, v64\n"
    "v_exp_f32 v65, v65\n"
    "v_exp_f32 v66, v66\n"
    "v_exp_f32 v67, v67\n"
    "v_mfma_f32_32x32x16_bf16 v[80:95], v[20:23], v[220:223], v[80:95]\n"
    "v_exp_f32 v68, v68\n"
    "v_exp_f32 v69, v69\n"
    "v_exp_f32 v70, v70\n"
    "v_exp_f32 v71, v71\n"
    "v_smfmac_f32_32x32x32_bf16 v[144:159], v[28:31], v[96:103], v104\n"
    "v_exp_f32 v96, v72\n"
    "v_exp_f32 v97, v73\n"
    "v_exp_f32 v98, v74\n"
    "v_exp_f32 v99, v75\n"
    "v_smfmac_f32_32x32x32_bf16 v[160:175], v[24:27], v[64:71], v104\n"
    "s_waitcnt vmcnt(4)\n"
    "v_exp_f32 v100, v76\n"
    "v_exp_f32 v101, v77\n"
    "v_exp_f32 v102, v78\n"
    "v_exp_f32 v103, v79\n"
    "v_mfma_f32_32x32x16_bf16 v[64:79], v[32:35], v[192:195], 0\n"
    "v_exp_f32 v80, v80\n"
    "v_exp_f32 v81, v81\n"
    "v_exp_f32 v82, v82\n"
    "v_exp_f32 v83, v83\n"
    "v_mfma_f32_32x32x16_bf16 v[64:79], v[36:39], v[196:199], v[64:79]\n"
    "v_exp_f32 v84, v84\n"
    "v_exp_f32 v85, v85\n"
    "v_exp_f32 v86, v86\n"
    "v_exp_f32 v87, v87\n"
    "v_smfmac_f32_32x32x32_bf16 v[160:175], v[28:31], v[96:103], v104\n"
    "v_exp_f32 v96, v88\n"
    "v_exp_f32 v97, v89\n"
    "v_exp_f32 v98, v90\n"
    "v_exp_f32 v99, v91\n"
    "v_smfmac_f32_32x32x32_bf16 v[176:191], v[24:27], v[80:87], v104\n"
    "v_exp_f32 v100, v92\n"
    "v_exp_f32 v101, v93\n"
    "v_exp_f32 v102, v94\n"
    "v_exp_f32 v103, v95\n"
    "v_mfma_f32_32x32x16_bf16 v[80:95], v[32:35], v[200:203], 0\n"
    "v_exp_f32 v64, v64\n"
    "v_exp_f32 v65, v65\n"
    "v_exp_f32 v66, v66\n"
    "v_exp_f32 v67, v67\n"
    "v_mfma_f32_32x32x16_bf16 v[80:95], v[36:39], v[204:207], v[80:95]\n"
    "v_exp_f32 v68, v68\n"
    "v_exp_f32 v69, v69\n"
    "v_exp_f32 v70, v70\n"
    "v_exp_f32 v71, v71\n"
    "v_smfmac_f32_32x32x32_bf16 v[176:191], v[28:31], v[96:103], v104\n"
    "v_exp_f32 v96, v72\n"
    "v_exp_f32 v97, v73\n"
    "v_exp_f32 v98, v74\n"
    "v_exp_f32 v99, v75\n"
    "v_smfmac_f32_32x32x32_bf16 v[128:143], v[40:43], v[64:71], v104\n"
    "v_exp_f32 v100, v76\n"
    "v_exp_f32 v101, v77\n"
    "v_exp_f32 v102, v78\n"
    "v_exp_f32 v103, v79\n"
    "v_mfma_f32_32x32x16_bf16 v[64:79], v[32:35], v[208:211], 0\n"
    "s_waitcnt lgkmcnt(0)\n"
    "v_readfirstlane_b32 s91, v127\n"
    "s_cmp_ge_u32 s91, 8\n"
    "s_cbranch_scc0 .Lgm_lc_39\n"
    ".Lgm_lcb_40:\n"
    "v_exp_f32 v80, v80\n"
    "v_exp_f32 v81, v81\n"
    "v_exp_f32 v82, v82\n"
    "v_exp_f32 v83, v83\n"
    "v_mfma_f32_32x32x16_bf16 v[64:79], v[36:39], v[212:215], v[64:79]\n"
    "v_exp_f32 v84, v84\n"
    "v_exp_f32 v85, v85\n"
    "v_exp_f32 v86, v86\n"
    "v_exp_f32 v87, v87\n"
    "v_smfmac_f32_32x32x32_bf16 v[128:143], v[44:47], v[96:103], v104\n"
    "v_exp_f32 v96, v88\n"
    "v_exp_f32 v97, v89\n"
    "v_exp_f32 v98, v90\n"
    "v_exp_f32 v99, v91\n"
    "v_smfmac_f32_32x32x32_bf16 v[144:159], v[40:43], v[80:87], v104\n"
    "v_exp_f32 v100, v92\n"
    "v_exp_f32 v101, v93\n"
    "v_exp_f32 v102, v94\n"
    "v_exp_f32 v103, v95\n"
    "v_mfma_f32_32x32x16_bf16 v[80:95], v[32:35], v[216:219], 0\n"
    "v_exp_f32 v64, v64\n"
    "v_exp_f32 v65, v65\n"
    "v_exp_f32 v66, v66\n"
    "v_exp_f32 v67, v67\n"
    "v_mfma_f32_32x32x16_bf16 v[80:95], v[36:39], v[220:223], v[80:95]\n"
    "v_exp_f32 v68, v68\n"
    "v_exp_f32 v69, v69\n"
    "v_exp_f32 v70, v70\n"
    "v_exp_f32 v71, v71\n"
    "v_smfmac_f32_32x32x32_bf16 v[144:159], v[44:47], v[96:103], v104\n"
    "v_exp_f32 v96, v72\n"
    "v_exp_f32 v97, v73\n"
    "v_exp_f32 v98, v74\n"
    "v_exp_f32 v99, v75\n"
    "v_smfmac_f32_32x32x32_bf16 v[160:175], v[40:43], v[64:71], v104\n"
    "s_waitcnt vmcnt(0)\n"
    "v_exp_f32 v100, v76\n"
    "v_exp_f32 v101, v77\n"
    "v_exp_f32 v102, v78\n"
    "v_exp_f32 v103, v79\n"
    "v_mfma_f32_32x32x16_bf16 v[64:79], v[48:51], v[192:195], 0\n"
    "v_exp_f32 v80, v80\n"
    "v_exp_f32 v81, v81\n"
    "v_exp_f32 v82, v82\n"
    "v_exp_f32 v83, v83\n"
    "v_mfma_f32_32x32x16_bf16 v[64:79], v[52:55], v[196:199], v[64:79]\n"
    "v_exp_f32 v84, v84\n"
    "v_exp_f32 v85, v85\n"
    "v_exp_f32 v86, v86\n"
    "v_exp_f32 v87, v87\n"
    "v_smfmac_f32_32x32x32_bf16 v[160:175], v[44:47], v[96:103], v104\n"
    "v_exp_f32 v96, v88\n"
    "v_exp_f32 v97, v89\n"
    "v_exp_f32 v98, v90\n"
    "v_exp_f32 v99, v91\n"
    "v_smfmac_f32_32x32x32_bf16 v[176:191], v[40:43], v[80:87], v104\n"
    "v_exp_f32 v100, v92\n"
    "v_exp_f32 v101, v93\n"
    "v_exp_f32 v102, v94\n"
    "v_exp_f32 v103, v95\n"
    "v_mfma_f32_32x32x16_bf16 v[80:95], v[48:51], v[200:203], 0\n"
    "v_exp_f32 v64, v64\n"
    "v_exp_f32 v65, v65\n"
    "v_exp_f32 v66, v66\n"
    "v_exp_f32 v67, v67\n"
    "v_mfma_f32_32x32x16_bf16 v[80:95], v[52:55], v[204:207], v[80:95]\n"
    "v_exp_f32 v68, v68\n"
    "v_exp_f32 v69, v69\n"
    "v_exp_f32 v70, v70\n"
    "v_exp_f32 v71, v71\n"
    "v_smfmac_f32_32x32x32_bf16 v[176:191], v[44:47], v[96:103], v104\n"
    "v_exp_f32 v96, v72\n"
    "v_exp_f32 v97, v73\n"
    "v_exp_f32 v98, v74\n"
    "v_exp_f32 v99, v75\n"
    "v_smfmac_f32_32x32x32_bf16 v[128:143], v[56:59], v[64:71], v104\n"
    "v_exp_f32 v100, v76\n"
    "v_exp_f32 v101, v77\n"
    "v_exp_f32 v102, v78\n"
    "v_exp_f32 v103, v79\n"
    "v_mfma_f32_32x32x16_bf16 v[64:79], v[48:51], v[208:211], 0\n"
    "v_exp_f32 v80, v80\n"
    "v_exp_f32 v81, v81\n"
    "v_exp_f32 v82, v82\n"
    "v_exp_f32 v83, v83\n"
    "v_mfma_f32_32x32x16_bf16 v[64:79], v[52:55], v[212:215], v[64:79]\n"
    "v_exp_f32 v84, v84\n"
    "v_exp_f32 v85, v85\n"
    "v_exp_f32 v86, v86\n"
    "v_exp_f32 v87, v87\n"
    "v_smfmac_f32_32x32x32_bf16 v[128:143], v[60:63], v[96:103], v104\n"
    "v_exp_f32 v96, v88\n"
    "v_exp_f32 v97, v89\n"
    "v_exp_f32 v98, v90\n"
    "v_exp_f32 v99, v91\n"
    "v_smfmac_f32_32x32x32_bf16 v[144:159], v[56:59], v[80:87], v104\n"
    "v_exp_f32 v100, v92\n"
    "v_exp_f32 v101, v93\n"
    "v_exp_f32 v102, v94\n"
    "v_exp_f32 v103, v95\n"
    "v_mfma_f32_32x32x16_bf16 v[80:95], v[48:51], v[216:219], 0\n"
    "s_mov_b64 exec, 1\n"
    "ds_add_u32 v126, v248 offset:48\n"
    "s_mov_b64 exec, -1\n"
    "v_exp_f32 v64, v64\n"
    "v_exp_f32 v65, v65\n"
    "v_exp_f32 v66, v66\n"
    "v_exp_f32 v67, v67\n"
    "ds_write_b128 v112, v[128:131] offset:0\n"
    "v_mfma_f32_32x32x16_bf16 v[80:95], v[52:55], v[220:223], v[80:95]\n"
    "ds_write_b128 v112, v[132:135] offset:1024\n"
    "v_exp_f32 v68, v68\n"
    "v_exp_f32 v69, v69\n"
    "v_exp_f32 v70, v70\n"
    "v_exp_f32 v71, v71\n"
    "v_smfmac_f32_32x32x32_bf16 v[144:159], v[60:63], v[96:103], v104\n"
    "ds_write_b128 v112, v[136:139] offset:2048\n"
    "v_exp_f32 v96, v72\n"
    "v_exp_f32 v97, v73\n"
    "v_exp_f32 v98, v74\n"
    "v_exp_f32 v99, v75\n"
    "v_smfmac_f32_32x32x32_bf16 v[160:175], v[56:59], v[64:71], v104\n"
    "ds_write_b128 v112, v[140:143] offset:3072\n"
    "v_exp_f32 v100, v76\n"
    "v_exp_f32 v101, v77\n"
    "v_exp_f32 v102, v78\n"
    "v_exp_f32 v103, v79\n"
    "v_exp_f32 v80, v80\n"
    "v_exp_f32 v81, v81\n"
    "v_exp_f32 v82, v82\n"
    "v_exp_f32 v83, v83\n"
    "ds_write_b128 v113, v[144:147] offset:4096\n"
    "ds_write_b128 v113, v[148:151] offset:5120\n"
    "v_exp_f32 v84, v84\n"
    "v_exp_f32 v85, v85\n"
    "v_exp_f32 v86, v86\n"
    "v_exp_f32 v87, v87\n"
    "v_smfmac_f32_32x32x32_bf16 v[160:175], v[60:63], v[96:103], v104\n"
    "ds_write_b128 v113, v[152:155] offset:6144\n"
    "v_exp_f32 v96, v88\n"
    "v_exp_f32 v97, v89\n"
    "v_exp_f32 v98, v90\n"
    "v_exp_f32 v99, v91\n"
    "v_smfmac_f32_32x32x32_bf16 v[176:191], v[56:59], v[80:87], v104\n"
    "ds_write_b128 v113, v[156:159] offset:7168\n"
    "v_exp_f32 v100, v92\n"
    "v_exp_f32 v101, v93\n"
    "v_exp_f32 v102, v94\n"
    "v_exp_f32 v103, v95\n"
    "s_nop 0\n"
    "ds_write_b128 v114, v[160:163] offset:8192\n"
    "ds_write_b128 v114, v[164:167] offset:9216\n"
    "v_smfmac_f32_32x32x32_bf16 v[176:191], v[60:63], v[96:103], v104\n"
    "ds_write_b128 v114, v[168:171] offset:10240\n"
    "ds_write_b128 v114, v[172:175] offset:11264\n"
    "s_mov_b64 exec, 1\n"
    "ds_add_u32 v249, v248 offset:16\n"
    "s_mov_b64 exec, -1\n"
    "s_mov_b32 s90, 1048576\n"
    ".Lgm_lw_41:\n"
    "ds_read_b32 v127, v249 offset:16\n"
    "s_waitcnt lgkmcnt(0)\n"
    "v_readfirstlane_b32 s91, v127\n"
    "s_cmp_ge_u32 s91, 4\n"
    "s_cbranch_scc1 .Lgm_lwd_42\n"
    "s_sub_u32 s90, s90, 1\n"
    "s_cbranch_scc0 .Lgm_lw_41\n"
    ".Lgm_lwd_42:\n"
    "ds_read_b128 v[12:15], v115 offset:3072\n"
    "ds_read_b128 v[28:31], v115 offset:7168\n"
    "ds_read_b128 v[44:47], v115 offset:11264\n"
    "ds_read_b128 v[0:3], v115 offset:0\n"
    "ds_read_b128 v[16:19], v115 offset:4096\n"
    "ds_read_b128 v[32:35], v115 offset:8192\n"
    "ds_read_b128 v[4:7], v115 offset:1024\n"
    "ds_read_b128 v[20:23], v115 offset:5120\n"
    "ds_read_b128 v[36:39], v115 offset:9216\n"
    "ds_read_b128 v[8:11], v115 offset:2048\n"
    "ds_read_b128 v[24:27], v115 offset:6144\n"
    "ds_read_b128 v[40:43], v115 offset:10240\n"
    "s_waitcnt lgkmcnt(9)\n"
    "v_add_f32 v188, v188, v12\n"
    "v_add_f32 v28, v28, v44\n"
    "v_add_f32 v189, v189, v13\n"
    "v_add_f32 v29, v29, v45\n"
    "v_add_f32 v190, v190, v14\n"
    "v_add_f32 v30, v30, v46\n"
    "v_add_f32 v191, v191, v15\n"
    "v_add_f32 v31, v31, v47\n"
    "v_add_f32 v188, v188, v28\n"
    "v_add_f32 v189, v189, v29\n"
    "v_add_f32 v190, v190, v30\n"
    "v_add_f32 v191, v191, v31\n"
    "v_mov_b32 v60, v188\n"
    "v_mov_b32 v61, v188\n"
    "s_nop 1\n"
    "v_permlane32_swap_b32 v60, v61\n"
    "v_subrev_u32 v62, 0x21800000, v61\n"
    "v_cmp_le_u32 vcc, 0x3c000000, v62\n"
    "v_rcp_f32 v62, v61\n"
    "s_waitcnt lgkmcnt(6)\n"
    "v_add_f32 v176, v176, v0\n"
    "v_add_f32 v16, v16, v32\n"
    "v_add_f32 v177, v177, v1\n"
    "v_add_f32 v17, v17, v33\n"
    "v_add_f32 v178, v178, v2\n"
    "v_add_f32 v18, v18, v34\n"
    "v_add_f32 v179, v179, v3\n"
    "v_add_f32 v19, v19, v35\n"
    "v_add_f32 v176, v176, v16\n"
    "v_add_f32 v177, v177, v17\n"
    "v_add_f32 v178, v178, v18\n"
    "v_add_f32 v179, v179, v19\n"
    "s_waitcnt lgkmcnt(3)\n"
    "v_add_f32 v180, v180, v4\n"
    "v_add_f32 v20, v20, v36\n"
    "v_add_f32 v181, v181, v5\n"
    "v_add_f32 v21, v21, v37\n"
    "v_add_f32 v182, v182, v6\n"
    "v_add_f32 v22, v22, v38\n"
    "v_add_f32 v183, v183, v7\n"
    "v_add_f32 v23, v23, v39\n"
    "v_add_f32 v180, v180, v20\n"
    "v_add_f32 v181, v181, v21\n"
    "v_add_f32 v182, v182, v22\n"
    "v_add_f32 v183, v183, v23\n"
    "s_waitcnt lgkmcnt(0)\n"
    "v_add_f32 v184, v184, v8\n"
    "v_add_f32 v24, v24, v40\n"
    "v_add_f32 v185, v185, v9\n"
    "v_add_f32 v25, v25, v41\n"
    "v_add_f32 v186, v186, v10\n"
    "v_add_f32 v26, v26, v42\n"
    "v_add_f32 v187, v187, v11\n"
    "v_add_f32 v27, v27, v43\n"
    "v_add_f32 v184, v184, v24\n"
    "v_add_f32 v185, v185, v25\n"
    "v_add_f32 v186, v186, v26\n"
    "v_add_f32 v187, v187, v27\n"
    "s_and_b64 vcc, exec, vcc\n"
    "s_cbranch_scc1 .Lgm_slow_43\n"
    ".Lgm_norm_44:\n"
    "v_mul_f32 v176, v62, v176\n"
    "v_mul_f32 v177, v62, v177\n"
    "v_mul_f32 v178, v62, v178\n"
    "v_mul_f32 v179, v62, v179\n"
    "v_mul_f32 v180, v62, v180\n"
    "v_mul_f32 v181, v62, v181\n"
    "v_mul_f32 v182, v62, v182\n"
    "v_mul_f32 v183, v62, v183\n"
    "v_cvt_pk_bf16_f32 v48, v176, v177\n"
    "v_cvt_pk_bf16_f32 v49, v178, v179\n"
    "v_cvt_pk_bf16_f32 v50, v180, v181\n"
    "v_cvt_pk_bf16_f32 v51, v182, v183\n"
    "buffer_store_dwordx4 v[48:51], v252, s[24:27], s51 offen offset:0 sc1\n"
    "v_mul_f32 v184, v62, v184\n"
    "v_mul_f32 v185, v62, v185\n"
    "v_mul_f32 v186, v62, v186\n"
    "v_mul_f32 v187, v62, v187\n"
    "v_mul_f32 v188, v62, v188\n"
    "v_mul_f32 v189, v62, v189\n"
    "v_mul_f32 v190, v62, v190\n"
    "v_mul_f32 v191, v62, v191\n"
    "v_cvt_pk_bf16_f32 v52, v184, v185\n"
    "v_cvt_pk_bf16_f32 v53, v186, v187\n"
    "v_cvt_pk_bf16_f32 v54, v188, v189\n"
    "v_cvt_pk_bf16_f32 v55, v190, v191\n"
    "v_and_b32 v54, v116, v54\n"
    "v_and_b32 v55, v116, v55\n"
    "buffer_store_dwordx4 v[52:55], v252, s[24:27], s51 offen offset:1024 sc1\n"
    "s_mov_b64 exec, 1\n"
    "ds_add_u32 v126, v248 offset:48\n"
    "s_mov_b64 exec, -1\n"
    "s_cmp_lt_u32 s46, 28\n"
    "s_cbranch_scc0 .Lgm_a_noc_45\n"
    "s_lshl_b32 s85, s68, 7\n"
    "v_lshrrev_b32 v21, 4, v255\n"
    "v_mov_b32 v118, v21\n"
    "v_and_b32 v119, 15, v255\n"
    "v_and_b32 v22, 1, v21\n"
    "v_lshl_add_u32 v23, v22, 2, v234\n"
    "v_lshl_add_u32 v23, v237, 4, v23\n"
    "v_lshlrev_b32 v23, 7, v23\n"
    "v_lshl_add_u32 v23, v22, 6, v23\n"
    "v_and_b32 v24, 8, v255\n"
    "v_lshl_add_u32 v23, v24, 2, v23\n"
    "v_and_b32 v24, 3, v255\n"
    "v_lshl_add_u32 v112, v24, 3, v23\n"
    "v_xor_b32 v113, 32, v112\n"
    "v_xor_b32 v114, 64, v112\n"
    "v_xor_b32 v115, 0x60, v112\n"
    "v_and_b32 v24, v22, v237\n"
    "v_lshlrev_b32 v117, 10, v24\n"
    "s_and_b32 s86, s44, 1\n"
    "s_lshl_b32 s86, s86, 8\n"
    "v_lshlrev_b32 v24, 4, v119\n"
    "v_lshl_add_u32 v24, v118, 9, v24\n"
    "v_add_u32 v116, s86, v24\n"
    "s_lshl_b32 s86, s44, 12\n"
    "s_add_u32 s86, s86, 57344\n"
    "v_add_u32 v120, s86, v252\n"
    "v_add_u32 v122, 0x12000, v120\n"
    "v_mul_u32_u24 v24, 896, v119\n"
    "v_lshl_add_u32 v24, v22, 5, v24\n"
    "v_lshl_add_u32 v121, v237, 4, v24\n"
    "s_lshr_b32 s86, s44, 1\n"
    "s_lshl_b32 s71, s67, 1\n"
    "s_add_u32 s71, s71, s86\n"
    "s_lshl_b32 s71, s71, 15\n"
    "s_add_u32 s71, s71, 13107200\n"
    "s_lshr_b32 s89, s67, 1\n"
    "s_lshl_b32 s89, s89, 11\n"
    "v_mov_b32 v123, v249\n"
    "s_add_u32 s89, s89, 65664\n"
    "v_lshlrev_b32 v25, 7, v255\n"
    "s_lshl_b32 s86, s67, 2\n"
    "s_add_u32 s86, s86, s44\n"
    "s_lshl_b32 s94, s44, 8\n"
    "s_add_u32 s94, s94, 156672\n"
    "s_mul_i32 s95, s86, 14336\n"
    "s_add_u32 s95, s95, s85\n"
    "v_lshl_add_u32 v125, v255, 2, s94\n"
    "s_mov_b32 s90, 1048576\n"
    ".Lgm_lw_46:\n"
    "ds_read_b32 v126, v249 offset:20\n"
    "s_waitcnt lgkmcnt(0)\n"
    "v_readfirstlane_b32 s91, v126\n"
    "s_cmp_ge_u32 s91, 4\n"
    "s_cbranch_scc1 .Lgm_lwd_47\n"
    "s_sub_u32 s90, s90, 1\n"
    "s_cbranch_scc0 .Lgm_lw_46\n"
    ".Lgm_lwd_47:\n"
    "ds_read_b32 v253, v125\n"
    "ds_read_b64_tr_b16 v[64:65], v112 offset:0\n"
    "ds_read_b64_tr_b16 v[68:69], v113 offset:0\n"
    "ds_read_b64_tr_b16 v[72:73], v114 offset:0\n"
    "ds_read_b64_tr_b16 v[76:77], v115 offset:0\n"
    "ds_read_b64_tr_b16 v[66:67], v112 offset:1024\n"
    "ds_read_b64_tr_b16 v[70:71], v113 offset:1024\n"
    "ds_read_b64_tr_b16 v[74:75], v114 offset:1024\n"
    "ds_read_b64_tr_b16 v[78:79], v115 offset:1024\n"
    "ds_read_b64_tr_b16 v[80:81], v114 offset:3584\n"
    "ds_read_b64_tr_b16 v[84:85], v115 offset:3584\n"
    "ds_read_b64_tr_b16 v[88:89], v112 offset:3584\n"
    "ds_read_b64_tr_b16 v[92:93], v113 offset:3584\n"
    "ds_read_b64_tr_b16 v[82:83], v114 offset:4608\n"
    "ds_read_b64_tr_b16 v[86:87], v115 offset:4608\n"
    "ds_read_b64_tr_b16 v[90:91], v112 offset:4608\n"
    "ds_read_b64_tr_b16 v[94:95], v113 offset:4608\n"
    "s_waitcnt vmcnt(0)\n"
    "s_mov_b64 exec, 1\n"
    "ds_add_rtn_u32 v26, v249, v248 offset:8\n"
    "s_waitcnt lgkmcnt(0)\n"
    "v_readfirstlane_b32 s91, v26\n"
    "s_cmp_eq_u32 s91, 3\n"
    "s_cbranch_scc0 .Lgm_arr_48\n"
    "buffer_store_dwordx2 v[250:251], off, s[24:27], s64 sc1\n"
    ".Lgm_arr_48:\n"
    "s_mov_b64 exec, -1\n"
    "s_mov_b32 s94, s91\n"
    "s_mul_i32 s84, s44, 24576\n"
    "s_add_u32 s84, s84, 73728\n"
    "v_lshrrev_b32 v28, 1, v118\n"
    "v_and_b32 v29, 7, v119\n"
    "v_xor_b32 v28, v28, v29\n"
    "v_lshlrev_b32 v28, 4, v28\n"
    "v_lshl_add_u32 v28, v119, 7, v28\n"
    "v_and_b32 v29, 1, v118\n"
    "v_lshl_add_u32 v28, v29, 3, v28\n"
    "v_add_u32 v118, s84, v28\n"
    "v_lshrrev_b32 v28, 3, v255\n"
    "v_and_b32 v29, 7, v255\n"
    "v_xor_b32 v29, v29, v28\n"
    "v_mul_u32_u24 v28, 896, v28\n"
    "v_lshl_add_u32 v121, v29, 4, v28\n"
    "v_xor_b32 v119, 32, v118\n"
    "v_xor_b32 v125, 64, v118\n"
    "v_xor_b32 v250, 96, v118\n"
    "v_add_u32 v251, s84, v252\n"
    "ds_read_b64_tr_b16 v[128:129], v112 offset:7168\n"
    "ds_read_b64_tr_b16 v[132:133], v113 offset:7168\n"
    "ds_read_b64_tr_b16 v[136:137], v114 offset:7168\n"
    "ds_read_b64_tr_b16 v[140:141], v115 offset:7168\n"
    "ds_read_b64_tr_b16 v[130:131], v112 offset:8192\n"
    "ds_read_b64_tr_b16 v[134:135], v113 offset:8192\n"
    "ds_read_b64_tr_b16 v[138:139], v114 offset:8192\n"
    "ds_read_b64_tr_b16 v[142:143], v115 offset:8192\n"
    "ds_read_b64_tr_b16 v[144:145], v114 offset:10752\n"
    "ds_read_b64_tr_b16 v[148:149], v115 offset:10752\n"
    "ds_read_b64_tr_b16 v[152:153], v112 offset:10752\n"
    "ds_read_b64_tr_b16 v[156:157], v113 offset:10752\n"
    "ds_read_b64_tr_b16 v[146:147], v114 offset:11776\n"
    "ds_read_b64_tr_b16 v[150:151], v115 offset:11776\n"
    "ds_read_b64_tr_b16 v[154:155], v112 offset:11776\n"
    "ds_read_b64_tr_b16 v[158:159], v113 offset:11776\n"
    "ds_read_b64_tr_b16 v[160:161], v112 offset:14336\n"
    "ds_read_b64_tr_b16 v[164:165], v113 offset:14336\n"
    "ds_read_b64_tr_b16 v[168:169], v114 offset:14336\n"
    "ds_read_b64_tr_b16 v[172:173], v115 offset:14336\n"
    "ds_read_b64_tr_b16 v[162:163], v112 offset:15360\n"
    "ds_read_b64_tr_b16 v[166:167], v113 offset:15360\n"
    "ds_read_b64_tr_b16 v[170:171], v114 offset:15360\n"
    "ds_read_b64_tr_b16 v[174:175], v115 offset:15360\n"
    "ds_read_b64_tr_b16 v[176:177], v114 offset:17920\n"
    "ds_read_b64_tr_b16 v[180:181], v115 offset:17920\n"
    "ds_read_b64_tr_b16 v[184:185], v112 offset:17920\n"
    "ds_read_b64_tr_b16 v[188:189], v113 offset:17920\n"
    "ds_read_b64_tr_b16 v[178:179], v114 offset:18944\n"
    "ds_read_b64_tr_b16 v[182:183], v115 offset:18944\n"
    "ds_read_b64_tr_b16 v[186:187], v112 offset:18944\n"
    "ds_read_b64_tr_b16 v[190:191], v113 offset:18944\n"
    "ds_read_b64_tr_b16 v[192:193], v112 offset:21504\n"
    "ds_read_b64_tr_b16 v[196:197], v113 offset:21504\n"
    "ds_read_b64_tr_b16 v[200:201], v114 offset:21504\n"
    "ds_read_b64_tr_b16 v[204:205], v115 offset:21504\n"
    "ds_read_b64_tr_b16 v[194:195], v112 offset:22528\n"
    "ds_read_b64_tr_b16 v[198:199], v113 offset:22528\n"
    "ds_read_b64_tr_b16 v[202:203], v114 offset:22528\n"
    "ds_read_b64_tr_b16 v[206:207], v115 offset:22528\n"
    "ds_read_b64_tr_b16 v[208:209], v114 offset:25088\n"
    "ds_read_b64_tr_b16 v[212:213], v115 offset:25088\n"
    "ds_read_b64_tr_b16 v[216:217], v112 offset:25088\n"
    "ds_read_b64_tr_b16 v[220:221], v113 offset:25088\n"
    "ds_read_b64_tr_b16 v[210:211], v114 offset:26112\n"
    "ds_read_b64_tr_b16 v[214:215], v115 offset:26112\n"
    "ds_read_b64_tr_b16 v[218:219], v112 offset:26112\n"
    "ds_read_b64_tr_b16 v[222:223], v113 offset:26112\n"
    "s_waitcnt vmcnt(0)\n"
    "s_cmp_eq_u32 s94, 0\n"
    "s_cbranch_scc0 .Lgm_op_49\n"
    "s_bfm_b64 exec, 16, 0\n"
    "s_mov_b32 s91, 65536\n"
    ".Lgm_poll_51:\n"
    "buffer_load_dwordx2 v[26:27], v25, s[24:27], s89 offen sc1\n"
    "s_waitcnt vmcnt(0)\n"
    "v_cmp_ne_u64 vcc, s[74:75], v[26:27]\n"
    "s_and_b64 vcc, exec, vcc\n"
    "s_cbranch_scc0 .Lgm_polld_52\n"
    "s_sleep 2\n"
    "s_sub_u32 s91, s91, 1\n"
    "s_cbranch_scc0 .Lgm_poll_51\n"
    ".Lgm_polld_52:\n"
    "s_mov_b64 exec, -1\n"
    "s_mov_b64 exec, 1\n"
    "ds_add_u32 v123, v248 offset:40\n"
    "s_mov_b64 exec, -1\n"
    "s_branch .Lgm_op_50\n"
    ".Lgm_op_49:\n"
    "s_mov_b32 s90, 1048576\n"
    ".Lgm_lw_53:\n"
    "ds_read_b32 v26, v123 offset:40\n"
    "s_waitcnt lgkmcnt(0)\n"
    "v_readfirstlane_b32 s91, v26\n"
    "s_cmp_ge_u32 s91, 1\n"
    "s_cbranch_scc1 .Lgm_lwd_54\n"
    "s_sub_u32 s90, s90, 1\n"
    "s_cbranch_scc0 .Lgm_lw_53\n"
    ".Lgm_lwd_54:\n"
    ".Lgm_op_50:\n"
    "ds_read_b32 v127, v249 offset:36\n"
    "buffer_load_dwordx4 v[0:3], v116, s[24:27], s71 offen offset:0 sc1\n"
    "buffer_load_dwordx4 v[4:7], v116, s[24:27], s71 offen offset:2048 sc1\n"
    "s_add_u32 s71, s71, 4096\n"
    "buffer_load_dwordx4 v[8:11], v116, s[24:27], s71 offen offset:0 sc1\n"
    "buffer_load_dwordx4 v[12:15], v116, s[24:27], s71 offen offset:2048 sc1\n"
    "s_add_u32 s71, s71, 4096\n"
    "buffer_load_dwordx4 v[16:19], v116, s[24:27], s71 offen offset:0 sc1\n"
    "buffer_load_dwordx4 v[20:23], v116, s[24:27], s71 offen offset:2048 sc1\n"
    "s_add_u32 s71, s71, 4096\n"
    "buffer_load_dwordx4 v[24:27], v116, s[24:27], s71 offen offset:0 sc1\n"
    "buffer_load_dwordx4 v[28:31], v116, s[24:27], s71 offen offset:2048 sc1\n"
    "s_add_u32 s71, s71, 4096\n"
    "buffer_load_dwordx4 v[32:35], v116, s[24:27], s71 offen offset:0 sc1\n"
    "buffer_load_dwordx4 v[36:39], v116, s[24:27], s71 offen offset:2048 sc1\n"
    "s_waitcnt lgkmcnt(0)\n"
    "v_readfirstlane_b32 s94, v127\n"
    "s_cmp_ge_u32 s94, 4\n"
    "s_cbranch_scc0 .Lgm_pl_55\n"
    "ds_read_b128 v[224:227], v120 offset:0\n"
    "ds_read_b128 v[228:231], v120 offset:1024\n"
    "ds_read_b128 v[232:235], v120 offset:2048\n"
    "ds_read_b128 v[236:239], v120 offset:3072\n"
    "ds_read_b128 v[40:43], v120 offset:24576\n"
    "ds_read_b128 v[44:47], v120 offset:25600\n"
    "ds_read_b128 v[48:51], v120 offset:26624\n"
    "ds_read_b128 v[52:55], v120 offset:27648\n"
    "ds_read_b128 v[240:243], v120 offset:49152\n"
    "ds_read_b128 v[244:247], v120 offset:50176\n"
    "ds_read_b128 v[56:59], v120 offset:51200\n"
    "ds_read_b128 v[60:63], v120 offset:52224\n"
    "v_sub_f32 v126, 1.0, v253\n"
    "s_waitcnt lgkmcnt(4)\n"
    "v_pk_add_f32 v[224:225], v[224:225], v[40:41]\n"
    "v_pk_add_f32 v[226:227], v[226:227], v[42:43]\n"
    "v_pk_add_f32 v[228:229], v[228:229], v[44:45]\n"
    "v_pk_add_f32 v[230:231], v[230:231], v[46:47]\n"
    "v_pk_add_f32 v[232:233], v[232:233], v[48:49]\n"
    "v_pk_add_f32 v[234:235], v[234:235], v[50:51]\n"
    "v_pk_add_f32 v[236:237], v[236:237], v[52:53]\n"
    "v_pk_add_f32 v[238:239], v[238:239], v[54:55]\n"
    "ds_read_b128 v[40:43], v122 offset:0\n"
    "ds_read_b128 v[44:47], v122 offset:1024\n"
    "ds_read_b128 v[48:51], v122 offset:2048\n"
    "ds_read_b128 v[52:55], v122 offset:3072\n"
    "s_waitcnt lgkmcnt(4)\n"
    "v_pk_add_f32 v[224:225], v[224:225], v[240:241]\n"
    "v_pk_add_f32 v[226:227], v[226:227], v[242:243]\n"
    "v_pk_add_f32 v[228:229], v[228:229], v[244:245]\n"
    "v_pk_add_f32 v[230:231], v[230:231], v[246:247]\n"
    "v_pk_add_f32 v[232:233], v[232:233], v[56:57]\n"
    "v_pk_add_f32 v[234:235], v[234:235], v[58:59]\n"
    "v_pk_add_f32 v[236:237], v[236:237], v[60:61]\n"
    "v_pk_add_f32 v[238:239], v[238:239], v[62:63]\n"
    "s_waitcnt lgkmcnt(0)\n"
    "v_pk_add_f32 v[224:225], v[224:225], v[40:41]\n"
    "v_pk_add_f32 v[226:227], v[226:227], v[42:43]\n"
    "v_pk_add_f32 v[228:229], v[228:229], v[44:45]\n"
    "v_pk_add_f32 v[230:231], v[230:231], v[46:47]\n"
    "v_pk_add_f32 v[232:233], v[232:233], v[48:49]\n"
    "v_pk_add_f32 v[234:235], v[234:235], v[50:51]\n"
    "v_pk_add_f32 v[236:237], v[236:237], v[52:53]\n"
    "v_pk_add_f32 v[238:239], v[238:239], v[54:55]\n"
    "v_pk_mul_f32 v[224:225], v[224:225], v[126:127] op_sel_hi:[1,0]\n"
    "v_pk_mul_f32 v[226:227], v[226:227], v[126:127] op_sel_hi:[1,0]\n"
    "v_pk_mul_f32 v[228:229], v[228:229], v[126:127] op_sel_hi:[1,0]\n"
    "v_pk_mul_f32 v[230:231], v[230:231], v[126:127] op_sel_hi:[1,0]\n"
    "v_pk_mul_f32 v[232:233], v[232:233], v[126:127] op_sel_hi:[1,0]\n"
    "v_pk_mul_f32 v[234:235], v[234:235], v[126:127] op_sel_hi:[1,0]\n"
    "v_pk_mul_f32 v[236:237], v[236:237], v[126:127] op_sel_hi:[1,0]\n"
    "v_pk_mul_f32 v[238:239], v[238:239], v[126:127] op_sel_hi:[1,0]\n"
    ".Lgm_pl_55:\n"
    "s_waitcnt vmcnt(9)\n"
    "v_mfma_f32_16x16x32_bf16 v[96:99], v[64:67], v[0:3], 0\n"
    "v_mfma_f32_16x16x32_bf16 v[100:103], v[68:71], v[0:3], 0\n"
    "v_mfma_f32_16x16x32_bf16 v[104:107], v[72:75], v[0:3], 0\n"
    "v_mfma_f32_16x16x32_bf16 v[108:111], v[76:79], v[0:3], 0\n"
    "s_add_u32 s71, s71, 4096\n"
    "buffer_load_dwordx4 v[40:43], v116, s[24:27], s71 offen offset:0 sc1\n"
    "s_waitcnt vmcnt(9)\n"
    "v_mfma_f32_16x16x32_bf16 v[96:99], v[80:83], v[4:7], v[96:99]\n"
    "ds_read_b64_tr_b16 v[64:65], v112 offset:28672\n"
    "ds_read_b64_tr_b16 v[68:69], v113 offset:28672\n"
    "v_mfma_f32_16x16x32_bf16 v[100:103], v[84:87], v[4:7], v[100:103]\n"
    "ds_read_b64_tr_b16 v[72:73], v114 offset:28672\n"
    "ds_read_b64_tr_b16 v[76:77], v115 offset:28672\n"
    "v_mfma_f32_16x16x32_bf16 v[104:107], v[88:91], v[4:7], v[104:107]\n"
    "ds_read_b64_tr_b16 v[66:67], v112 offset:29696\n"
    "ds_read_b64_tr_b16 v[70:71], v113 offset:29696\n"
    "v_mfma_f32_16x16x32_bf16 v[108:111], v[92:95], v[4:7], v[108:111]\n"
    "ds_read_b64_tr_b16 v[74:75], v114 offset:29696\n"
    "ds_read_b64_tr_b16 v[78:79], v115 offset:29696\n"
    "buffer_load_dwordx4 v[44:47], v116, s[24:27], s71 offen offset:2048 sc1\n"
    "s_waitcnt vmcnt(9)\n"
    "v_mfma_f32_16x16x32_bf16 v[96:99], v[128:131], v[8:11], v[96:99]\n"
    "ds_read_b64_tr_b16 v[80:81], v114 offset:32256\n"
    "ds_read_b64_tr_b16 v[84:85], v115 offset:32256\n"
    "v_mfma_f32_16x16x32_bf16 v[100:103], v[132:135], v[8:11], v[100:103]\n"
    "ds_read_b64_tr_b16 v[88:89], v112 offset:32256\n"
    "ds_read_b64_tr_b16 v[92:93], v113 offset:32256\n"
    "v_mfma_f32_16x16x32_bf16 v[104:107], v[136:139], v[8:11], v[104:107]\n"
    "ds_read_b64_tr_b16 v[82:83], v114 offset:33280\n"
    "ds_read_b64_tr_b16 v[86:87], v115 offset:33280\n"
    "v_mfma_f32_16x16x32_bf16 v[108:111], v[140:143], v[8:11], v[108:111]\n"
    "ds_read_b64_tr_b16 v[90:91], v112 offset:33280\n"
    "ds_read_b64_tr_b16 v[94:95], v113 offset:33280\n"
    "s_add_u32 s71, s71, 4096\n"
    "buffer_load_dwordx4 v[48:51], v116, s[24:27], s71 offen offset:0 sc1\n"
    "s_waitcnt vmcnt(9)\n"
    "v_mfma_f32_16x16x32_bf16 v[96:99], v[144:147], v[12:15], v[96:99]\n"
    "ds_read_b64_tr_b16 v[128:129], v112 offset:35840\n"
    "ds_read_b64_tr_b16 v[132:133], v113 offset:35840\n"
    "v_mfma_f32_16x16x32_bf16 v[100:103], v[148:151], v[12:15], v[100:103]\n"
    "ds_read_b64_tr_b16 v[136:137], v114 offset:35840\n"
    "ds_read_b64_tr_b16 v[140:141], v115 offset:35840\n"
    "v_mfma_f32_16x16x32_bf16 v[104:107], v[152:155], v[12:15], v[104:107]\n"
    "ds_read_b64_tr_b16 v[130:131], v112 offset:36864\n"
    "ds_read_b64_tr_b16 v[134:135], v113 offset:36864\n"
    "v_mfma_f32_16x16x32_bf16 v[108:111], v[156:159], v[12:15], v[108:111]\n"
    "ds_read_b64_tr_b16 v[138:139], v114 offset:36864\n"
    "ds_read_b64_tr_b16 v[142:143], v115 offset:36864\n"
    "buffer_load_dwordx4 v[52:55], v116, s[24:27], s71 offen offset:2048 sc1\n"
    "s_waitcnt vmcnt(9)\n"
    "v_mfma_f32_16x16x32_bf16 v[96:99], v[160:163], v[16:19], v[96:99]\n"
    "ds_read_b64_tr_b16 v[144:145], v114 offset:39424\n"
    "ds_read_b64_tr_b16 v[148:149], v115 offset:39424\n"
    "v_mfma_f32_16x16x32_bf16 v[100:103], v[164:167], v[16:19], v[100:103]\n"
    "ds_read_b64_tr_b16 v[152:153], v112 offset:39424\n"
    "ds_read_b64_tr_b16 v[156:157], v113 offset:39424\n"
    "v_mfma_f32_16x16x32_bf16 v[104:107], v[168:171], v[16:19], v[104:107]\n"
    "ds_read_b64_tr_b16 v[146:147], v114 offset:40448\n"
    "ds_read_b64_tr_b16 v[150:151], v115 offset:40448\n"
    "v_mfma_f32_16x16x32_bf16 v[108:111], v[172:175], v[16:19], v[108:111]\n"
    "ds_read_b64_tr_b16 v[154:155], v112 offset:40448\n"
    "ds_read_b64_tr_b16 v[158:159], v113 offset:40448\n"
    "s_add_u32 s71, s71, 4096\n"
    "buffer_load_dwordx4 v[56:59], v116, s[24:27], s71 offen offset:0 sc1\n"
    "s_waitcnt vmcnt(9)\n"
    "v_mfma_f32_16x16x32_bf16 v[96:99], v[176:179], v[20:23], v[96:99]\n"
    "ds_read_b64_tr_b16 v[160:161], v112 offset:43008\n"
    "ds_read_b64_tr_b16 v[164:165], v113 offset:43008\n"
    "v_mfma_f32_16x16x32_bf16 v[100:103], v[180:183], v[20:23], v[100:103]\n"
    "ds_read_b64_tr_b16 v[168:169], v114 offset:43008\n"
    "ds_read_b64_tr_b16 v[172:173], v115 offset:43008\n"
    "v_mfma_f32_16x16x32_bf16 v[104:107], v[184:187], v[20:23], v[104:107]\n"
    "ds_read_b64_tr_b16 v[162:163], v112 offset:44032\n"
    "ds_read_b64_tr_b16 v[166:167], v113 offset:44032\n"
    "v_mfma_f32_16x16x32_bf16 v[108:111], v[188:191], v[20:23], v[108:111]\n"
    "ds_read_b64_tr_b16 v[170:171], v114 offset:44032\n"
    "ds_read_b64_tr_b16 v[174:175], v115 offset:44032\n"
    "buffer_load_dwordx4 v[60:63], v116, s[24:27], s71 offen offset:2048 sc1\n"
    "s_waitcnt vmcnt(9)\n"
    "v_mfma_f32_16x16x32_bf16 v[96:99], v[192:195], v[24:27], v[96:99]\n"
    "ds_read_b64_tr_b16 v[176:177], v114 offset:46592\n"
    "ds_read_b64_tr_b16 v[180:181], v115 offset:46592\n"
    "v_mfma_f32_16x16x32_bf16 v[100:103], v[196:199], v[24:27], v[100:103]\n"
    "ds_read_b64_tr_b16 v[184:185], v112 offset:46592\n"
    "ds_read_b64_tr_b16 v[188:189], v113 offset:46592\n"
    "v_mfma_f32_16x16x32_bf16 v[104:107], v[200:203], v[24:27], v[104:107]\n"
    "ds_read_b64_tr_b16 v[178:179], v114 offset:47616\n"
    "ds_read_b64_tr_b16 v[182:183], v115 offset:47616\n"
    "v_mfma_f32_16x16x32_bf16 v[108:111], v[204:207], v[24:27], v[108:111]\n"
    "ds_read_b64_tr_b16 v[186:187], v112 offset:47616\n"
    "ds_read_b64_tr_b16 v[190:191], v113 offset:47616\n"
    "s_waitcnt vmcnt(8)\n"
    "v_mfma_f32_16x16x32_bf16 v[96:99], v[208:211], v[28:31], v[96:99]\n"
    "ds_read_b64_tr_b16 v[192:193], v112 offset:50176\n"
    "ds_read_b64_tr_b16 v[196:197], v113 offset:50176\n"
    "v_mfma_f32_16x16x32_bf16 v[100:103], v[212:215], v[28:31], v[100:103]\n"
    "ds_read_b64_tr_b16 v[200:201], v114 offset:50176\n"
    "ds_read_b64_tr_b16 v[204:205], v115 offset:50176\n"
    "v_mfma_f32_16x16x32_bf16 v[104:107], v[216:219], v[28:31], v[104:107]\n"
    "ds_read_b64_tr_b16 v[194:195], v112 offset:51200\n"
    "ds_read_b64_tr_b16 v[198:199], v113 offset:51200\n"
    "v_mfma_f32_16x16x32_bf16 v[108:111], v[220:223], v[28:31], v[108:111]\n"
    "ds_read_b64_tr_b16 v[202:203], v114 offset:51200\n"
    "ds_read_b64_tr_b16 v[206:207], v115 offset:51200\n"
    "s_waitcnt vmcnt(7)\n"
    "s_waitcnt lgkmcnt(15)\n"
    "v_mfma_f32_16x16x32_bf16 v[96:99], v[64:67], v[32:35], v[96:99]\n"
    "ds_read_b64_tr_b16 v[208:209], v114 offset:53760\n"
    "ds_read_b64_tr_b16 v[212:213], v115 offset:53760\n"
    "v_mfma_f32_16x16x32_bf16 v[100:103], v[68:71], v[32:35], v[100:103]\n"
    "ds_read_b64_tr_b16 v[216:217], v112 offset:53760\n"
    "ds_read_b64_tr_b16 v[220:221], v113 offset:53760\n"
    "v_mfma_f32_16x16x32_bf16 v[104:107], v[72:75], v[32:35], v[104:107]\n"
    "v_sub_u32 v112, v112, v117\n"
    "v_sub_u32 v113, v113, v117\n"
    "v_sub_u32 v114, v114, v117\n"
    "v_sub_u32 v115, v115, v117\n"
    "ds_read_b64_tr_b16 v[210:211], v114 offset:54784\n"
    "v_mfma_f32_16x16x32_bf16 v[108:111], v[76:79], v[32:35], v[108:111]\n"
    "ds_read_b64_tr_b16 v[214:215], v115 offset:54784\n"
    "ds_read_b64_tr_b16 v[218:219], v112 offset:54784\n"
    "ds_read_b64_tr_b16 v[222:223], v113 offset:54784\n"
    "s_waitcnt vmcnt(6)\n"
    "v_mfma_f32_16x16x32_bf16 v[96:99], v[80:83], v[36:39], v[96:99]\n"
    "v_mfma_f32_16x16x32_bf16 v[100:103], v[84:87], v[36:39], v[100:103]\n"
    "v_mfma_f32_16x16x32_bf16 v[104:107], v[88:91], v[36:39], v[104:107]\n"
    "v_mfma_f32_16x16x32_bf16 v[108:111], v[92:95], v[36:39], v[108:111]\n"
    "s_waitcnt vmcnt(5)\n"
    "v_mfma_f32_16x16x32_bf16 v[96:99], v[128:131], v[40:43], v[96:99]\n"
    "v_mfma_f32_16x16x32_bf16 v[100:103], v[132:135], v[40:43], v[100:103]\n"
    "v_mfma_f32_16x16x32_bf16 v[104:107], v[136:139], v[40:43], v[104:107]\n"
    "v_mfma_f32_16x16x32_bf16 v[108:111], v[140:143], v[40:43], v[108:111]\n"
    "s_waitcnt vmcnt(4)\n"
    "v_mfma_f32_16x16x32_bf16 v[96:99], v[144:147], v[44:47], v[96:99]\n"
    "v_mfma_f32_16x16x32_bf16 v[100:103], v[148:151], v[44:47], v[100:103]\n"
    "v_mfma_f32_16x16x32_bf16 v[104:107], v[152:155], v[44:47], v[104:107]\n"
    "v_mfma_f32_16x16x32_bf16 v[108:111], v[156:159], v[44:47], v[108:111]\n"
    "s_waitcnt vmcnt(3)\n"
    "v_mfma_f32_16x16x32_bf16 v[96:99], v[160:163], v[48:51], v[96:99]\n"
    "v_mfma_f32_16x16x32_bf16 v[100:103], v[164:167], v[48:51], v[100:103]\n"
    "v_mfma_f32_16x16x32_bf16 v[104:107], v[168:171], v[48:51], v[104:107]\n"
    "v_mfma_f32_16x16x32_bf16 v[108:111], v[172:175], v[48:51], v[108:111]\n"
    "s_waitcnt vmcnt(2)\n"
    "s_waitcnt lgkmcnt(15)\n"
    "v_mfma_f32_16x16x32_bf16 v[96:99], v[176:179], v[52:55], v[96:99]\n"
    "v_mfma_f32_16x16x32_bf16 v[100:103], v[180:183], v[52:55], v[100:103]\n"
    "v_mfma_f32_16x16x32_bf16 v[104:107], v[184:187], v[52:55], v[104:107]\n"
    "v_mfma_f32_16x16x32_bf16 v[108:111], v[188:191], v[52:55], v[108:111]\n"
    "s_waitcnt vmcnt(1)\n"
    "s_waitcnt lgkmcnt(8)\n"
    "v_mfma_f32_16x16x32_bf16 v[96:99], v[192:195], v[56:59], v[96:99]\n"
    "v_mfma_f32_16x16x32_bf16 v[100:103], v[196:199], v[56:59], v[100:103]\n"
    "v_mfma_f32_16x16x32_bf16 v[104:107], v[200:203], v[56:59], v[104:107]\n"
    "v_mfma_f32_16x16x32_bf16 v[108:111], v[204:207], v[56:59], v[108:111]\n"
    "s_waitcnt vmcnt(0)\n"
    "s_waitcnt lgkmcnt(0)\n"
    "v_mfma_f32_16x16x32_bf16 v[96:99], v[208:211], v[60:63], v[96:99]\n"
    "v_mfma_f32_16x16x32_bf16 v[100:103], v[212:215], v[60:63], v[100:103]\n"
    "v_mfma_f32_16x16x32_bf16 v[104:107], v[216:219], v[60:63], v[104:107]\n"
    "v_mfma_f32_16x16x32_bf16 v[108:111], v[220:223], v[60:63], v[108:111]\n"
    "v_mov_b32 v2, v253\n"
    "s_cmp_ge_u32 s94, 4\n"
    "s_cbranch_scc0 .Lgm_p2_56\n"
    ".Lgm_p3_57:\n"
    "s_nop 1\n"
    "v_pk_fma_f32 v[96:97], v[96:97], v[2:3], v[224:225] op_sel_hi:[1,0,1]\n"
    "v_pk_fma_f32 v[98:99], v[98:99], v[2:3], v[226:227] op_sel_hi:[1,0,1]\n"
    "v_pk_fma_f32 v[100:101], v[100:101], v[2:3], v[228:229] op_sel_hi:[1,0,1]\n"
    "v_pk_fma_f32 v[102:103], v[102:103], v[2:3], v[230:231] op_sel_hi:[1,0,1]\n"
    "v_pk_fma_f32 v[104:105], v[104:105], v[2:3], v[232:233] op_sel_hi:[1,0,1]\n"
    "v_pk_fma_f32 v[106:107], v[106:107], v[2:3], v[234:235] op_sel_hi:[1,0,1]\n"
    "v_pk_fma_f32 v[108:109], v[108:109], v[2:3], v[236:237] op_sel_hi:[1,0,1]\n"
    "v_pk_fma_f32 v[110:111], v[110:111], v[2:3], v[238:239] op_sel_hi:[1,0,1]\n"
    "v_cvt_pk_bf16_f32 v8, v96, v97\n"
    "v_cvt_pk_bf16_f32 v9, v98, v99\n"
    "v_cvt_pk_bf16_f32 v10, v100, v101\n"
    "v_cvt_pk_bf16_f32 v11, v102, v103\n"
    "v_cvt_pk_bf16_f32 v12, v104, v105\n"
    "v_cvt_pk_bf16_f32 v13, v106, v107\n"
    "v_cvt_pk_bf16_f32 v14, v108, v109\n"
    "v_cvt_pk_bf16_f32 v15, v110, v111\n"
    "ds_write_b64 v118, v[8:9]\n"
    "ds_write_b64 v119, v[10:11]\n"
    "ds_write_b64 v125, v[12:13]\n"
    "ds_write_b64 v250, v[14:15]\n"
    "ds_read_b128 v[16:19], v251\n"
    "ds_read_b128 v[20:23], v251 offset:1024\n"
    "s_add_u32 s96, s95, 7168\n"
    "s_waitcnt lgkmcnt(1)\n"
    "buffer_store_dwordx4 v[16:19], v121, s[40:43], s95 offen sc1\n"
    "s_waitcnt lgkmcnt(0)\n"
    "buffer_store_dwordx4 v[20:23], v121, s[40:43], s96 offen sc1\n"
    "s_endpgm\n"
    ".Lgm_p2_56:\n"
    "s_mov_b32 s90, 1048576\n"
    ".Lgm_lw_58:\n"
    "ds_read_b32 v127, v249 offset:36\n"
    "s_waitcnt lgkmcnt(0)\n"
    "v_readfirstlane_b32 s91, v127\n"
    "s_cmp_ge_u32 s91, 4\n"
    "s_cbranch_scc1 .Lgm_lwd_59\n"
    "s_sub_u32 s90, s90, 1\n"
    "s_cbranch_scc0 .Lgm_lw_58\n"
    ".Lgm_lwd_59:\n"
    "ds_read_b128 v[224:227], v120 offset:0\n"
    "ds_read_b128 v[228:231], v120 offset:1024\n"
    "ds_read_b128 v[232:235], v120 offset:2048\n"
    "ds_read_b128 v[236:239], v120 offset:3072\n"
    "ds_read_b128 v[40:43], v120 offset:24576\n"
    "ds_read_b128 v[44:47], v120 offset:25600\n"
    "ds_read_b128 v[48:51], v120 offset:26624\n"
    "ds_read_b128 v[52:55], v120 offset:27648\n"
    "ds_read_b128 v[240:243], v120 offset:49152\n"
    "ds_read_b128 v[244:247], v120 offset:50176\n"
    "ds_read_b128 v[56:59], v120 offset:51200\n"
    "ds_read_b128 v[60:63], v120 offset:52224\n"
    "v_sub_f32 v126, 1.0, v253\n"
    "s_waitcnt lgkmcnt(4)\n"
    "v_pk_add_f32 v[224:225], v[224:225], v[40:41]\n"
    "v_pk_add_f32 v[226:227], v[226:227], v[42:43]\n"
    "v_pk_add_f32 v[228:229], v[228:229], v[44:45]\n"
    "v_pk_add_f32 v[230:231], v[230:231], v[46:47]\n"
    "v_pk_add_f32 v[232:233], v[232:233], v[48:49]\n"
    "v_pk_add_f32 v[234:235], v[234:235], v[50:51]\n"
    "v_pk_add_f32 v[236:237], v[236:237], v[52:53]\n"
    "v_pk_add_f32 v[238:239], v[238:239], v[54:55]\n"
    "ds_read_b128 v[40:43], v122 offset:0\n"
    "ds_read_b128 v[44:47], v122 offset:1024\n"
    "ds_read_b128 v[48:51], v122 offset:2048\n"
    "ds_read_b128 v[52:55], v122 offset:3072\n"
    "s_waitcnt lgkmcnt(4)\n"
    "v_pk_add_f32 v[224:225], v[224:225], v[240:241]\n"
    "v_pk_add_f32 v[226:227], v[226:227], v[242:243]\n"
    "v_pk_add_f32 v[228:229], v[228:229], v[244:245]\n"
    "v_pk_add_f32 v[230:231], v[230:231], v[246:247]\n"
    "v_pk_add_f32 v[232:233], v[232:233], v[56:57]\n"
    "v_pk_add_f32 v[234:235], v[234:235], v[58:59]\n"
    "v_pk_add_f32 v[236:237], v[236:237], v[60:61]\n"
    "v_pk_add_f32 v[238:239], v[238:239], v[62:63]\n"
    "s_waitcnt lgkmcnt(0)\n"
    "v_pk_add_f32 v[224:225], v[224:225], v[40:41]\n"
    "v_pk_add_f32 v[226:227], v[226:227], v[42:43]\n"
    "v_pk_add_f32 v[228:229], v[228:229], v[44:45]\n"
    "v_pk_add_f32 v[230:231], v[230:231], v[46:47]\n"
    "v_pk_add_f32 v[232:233], v[232:233], v[48:49]\n"
    "v_pk_add_f32 v[234:235], v[234:235], v[50:51]\n"
    "v_pk_add_f32 v[236:237], v[236:237], v[52:53]\n"
    "v_pk_add_f32 v[238:239], v[238:239], v[54:55]\n"
    "v_pk_mul_f32 v[224:225], v[224:225], v[126:127] op_sel_hi:[1,0]\n"
    "v_pk_mul_f32 v[226:227], v[226:227], v[126:127] op_sel_hi:[1,0]\n"
    "v_pk_mul_f32 v[228:229], v[228:229], v[126:127] op_sel_hi:[1,0]\n"
    "v_pk_mul_f32 v[230:231], v[230:231], v[126:127] op_sel_hi:[1,0]\n"
    "v_pk_mul_f32 v[232:233], v[232:233], v[126:127] op_sel_hi:[1,0]\n"
    "v_pk_mul_f32 v[234:235], v[234:235], v[126:127] op_sel_hi:[1,0]\n"
    "v_pk_mul_f32 v[236:237], v[236:237], v[126:127] op_sel_hi:[1,0]\n"
    "v_pk_mul_f32 v[238:239], v[238:239], v[126:127] op_sel_hi:[1,0]\n"
    "s_branch .Lgm_p3_57\n"
    ".Lgm_spec_27:\n"
    "s_waitcnt lgkmcnt(0)\n"
    "s_barrier\n"
    "ds_read_b64 v[250:251], v249 offset:64\n"
    "s_waitcnt lgkmcnt(0)\n"
    "s_sleep 110\n"
    "s_mov_b32 s91, s44\n"
    "s_branch .Lgm_e2_28\n"
    ".Lgm_lc_10:\n"
    "s_mov_b32 s90, 1048576\n"
    ".Lgm_lw_60:\n"
    "ds_read_b32 v228, v249 offset:24\n"
    "s_waitcnt lgkmcnt(0)\n"
    "v_readfirstlane_b32 s91, v228\n"
    "s_cmp_ge_u32 s91, 4\n"
    "s_cbranch_scc1 .Lgm_lwd_61\n"
    "s_sub_u32 s90, s90, 1\n"
    "s_cbranch_scc0 .Lgm_lw_60\n"
    ".Lgm_lwd_61:\n"
    "s_branch .Lgm_lcb_11\n"
    ".Lgm_lc_12:\n"
    "s_mov_b32 s90, 1048576\n"
    ".Lgm_lw_62:\n"
    "ds_read_b32 v229, v249 offset:28\n"
    "s_waitcnt lgkmcnt(0)\n"
    "v_readfirstlane_b32 s91, v229\n"
    "s_cmp_ge_u32 s91, 4\n"
    "s_cbranch_scc1 .Lgm_lwd_63\n"
    "s_sub_u32 s90, s90, 1\n"
    "s_cbranch_scc0 .Lgm_lw_62\n"
    ".Lgm_lwd_63:\n"
    "s_branch .Lgm_lcb_13\n"
    ".Lgm_b_idle_7:\n"
    "v_lshlrev_b32 v252, 4, v255\n"
    "v_and_b32 v236, 31, v255\n"
    "v_lshrrev_b32 v237, 5, v255\n"
    "v_bfe_u32 v234, v255, 2, 2\n"
    "v_mov_b32 v249, 158720\n"
    "v_mov_b32 v248, 1\n"
    "s_lshl_b32 s63, s47, 2\n"
    "s_add_u32 s63, s63, s59\n"
    "s_lshl_b32 s64, s47, 4\n"
    "s_add_u32 s64, s64, s48\n"
    "s_lshl_b32 s64, s64, 7\n"
    "s_add_u32 s64, s64, 128\n"
    "s_add_u32 s73, s64, 32768\n"
    "s_mul_i32 s84, s46, 37\n"
    "s_lshr_b32 s84, s84, 8\n"
    "s_mul_i32 s85, s84, 7\n"
    "s_sub_u32 s68, s46, s85\n"
    "s_lshl_b32 s67, s45, 2\n"
    "s_add_u32 s67, s67, s84\n"
    "s_mov_b32 s82, 0x3fb8aa3b\n"
    "s_mov_b32 s83, 0x3fb504f3\n"
    "v_lshl_add_u32 v2, v237, 3, v234\n"
    "v_lshlrev_b32 v2, 8, v2\n"
    "v_and_b32 v4, 16, v255\n"
    "v_lshl_add_u32 v2, v4, 1, v2\n"
    "v_and_b32 v4, 1, v255\n"
    "v_lshl_add_u32 v2, v4, 4, v2\n"
    "v_and_b32 v4, 2, v255\n"
    "v_lshl_add_u32 v233, v4, 2, v2\n"
    "s_lshl_b32 s84, s59, 12\n"
    "v_bfe_u32 v2, v255, 1, 3\n"
    "v_xor_b32 v2, v2, v237\n"
    "v_lshlrev_b32 v2, 4, v2\n"
    "v_lshl_add_u32 v2, v236, 7, v2\n"
    "v_add_u32 v224, s84, v2\n"
    "v_xor_b32 v225, 32, v224\n"
    "v_xor_b32 v226, 64, v224\n"
    "v_xor_b32 v227, 0x60, v224\n"
    "s_waitcnt lgkmcnt(0)\n"
    "buffer_load_dwordx2 v[250:251], off, s[24:27], 0 sc1\n"
    "s_lshl_b32 s53, s59, 10\n"
    "s_mov_b32 m0, s53\n"
    "s_nop 0\n"
    "buffer_load_dwordx4 v252, s[36:39], 0 offen lds\n"
    "s_and_b32 s84, s46, 3\n"
    "s_lshl_b32 s84, s84, 2\n"
    "s_add_u32 s94, s84, s59\n"
    "s_waitcnt vmcnt(0)\n"
    "v_readfirstlane_b32 s74, v250\n"
    "v_readfirstlane_b32 s75, v251\n"
    "s_add_u32 s74, s74, 0x7f4a7c15\n"
    "s_addc_u32 s75, s75, 0x9e3779b9\n"
    "v_mov_b32 v250, s74\n"
    "v_mov_b32 v251, s75\n"
    "ds_write_b64 v249, v[250:251] offset:64\n"
    "s_waitcnt lgkmcnt(0)\n"
    "s_barrier\n"
    "s_mov_b64 exec, 1\n"
    "ds_add_u32 v249, v248 offset:12\n"
    "s_mov_b64 exec, -1\n"
    "s_lshl_b32 s94, s94, 3\n"
    "s_add_u32 s94, s94, s45\n"
    "s_lshl_b32 s94, s94, 10\n"
    "s_mul_i32 s84, s94, 3\n"
    "s_lshr_b32 s84, s84, 2\n"
    "s_bfm_b64 exec, 48, 0\n"
    "buffer_load_dwordx4 v[4:7], v252, s[24:27], s84 offen offset:128 sc1\n"
    "s_mov_b64 exec, -1\n"
    "s_mov_b32 s90, 1048576\n"
    ".Lgm_lw_65:\n"
    "ds_read_b32 v20, v249 offset:44\n"
    "s_waitcnt lgkmcnt(0)\n"
    "v_readfirstlane_b32 s91, v20\n"
    "s_cmp_ge_u32 s91, 4\n"
    "s_cbranch_scc1 .Lgm_lwd_66\n"
    "s_sleep 8\n"
    "s_sub_u32 s90, s90, 1\n"
    "s_cbranch_scc0 .Lgm_lw_65\n"
    ".Lgm_lwd_66:\n"
    "s_mul_i32 s84, s94, 4\n"
    "s_mov_b32 s70, 4\n"
    ".Lgm_sw_67:\n"
    "buffer_load_dwordx4 v[4:7], v252, s[32:35], s84 offen\n"
    "s_add_u32 s84, s84, 1024\n"
    "s_waitcnt vmcnt(3)\n"
    "s_sub_u32 s70, s70, 1\n"
    "s_cmp_lg_u32 s70, 0\n"
    "s_cbranch_scc1 .Lgm_sw_67\n"
    "s_lshl_b32 s84, s47, 1\n"
    "s_add_u32 s84, s84, s60\n"
    "s_lshl_b32 s84, s84, 2\n"
    "s_add_u32 s84, s84, s59\n"
    "s_mul_i32 s85, s84, 14336\n"
    "s_lshl_b32 s84, s84, 6\n"
    "s_add_u32 s84, s84, 98560\n"
    "v_and_b32 v120, 48, v252\n"
    "v_lshrrev_b32 v121, 2, v255\n"
    "v_mul_u32_u24 v121, 896, v121\n"
    "v_add_u32 v121, v121, v120\n"
    "v_add_u32 v120, s53, v120\n"
    "v_mov_b32 v122, 0\n"
    "v_mov_b32 v123, 0\n"
    "buffer_load_dwordx4 v[8:11], v121, s[8:11], s85 offen offset:0\n"
    "buffer_load_dwordx4 v[12:15], v121, s[8:11], s85 offen offset:64\n"
    "buffer_load_dwordx4 v[16:19], v121, s[8:11], s85 offen offset:128\n"
    "buffer_load_dwordx4 v[20:23], v121, s[8:11], s85 offen offset:192\n"
    "buffer_load_dwordx4 v[24:27], v121, s[8:11], s85 offen offset:256\n"
    "buffer_load_dwordx4 v[28:31], v121, s[8:11], s85 offen offset:320\n"
    "buffer_load_dwordx4 v[32:35], v121, s[8:11], s85 offen offset:384\n"
    "buffer_load_dwordx4 v[36:39], v121, s[8:11], s85 offen offset:448\n"
    "buffer_load_dwordx4 v[40:43], v121, s[8:11], s85 offen offset:512\n"
    "buffer_load_dwordx4 v[44:47], v121, s[8:11], s85 offen offset:576\n"
    "buffer_load_dwordx4 v[48:51], v121, s[8:11], s85 offen offset:640\n"
    "buffer_load_dwordx4 v[52:55], v121, s[8:11], s85 offen offset:704\n"
    "buffer_load_dwordx4 v[56:59], v121, s[8:11], s85 offen offset:768\n"
    "buffer_load_dwordx4 v[60:63], v121, s[8:11], s85 offen offset:832\n"
    "ds_read_b128 v[128:131], v120 offset:0\n"
    "ds_read_b128 v[132:135], v120 offset:64\n"
    "ds_read_b128 v[136:139], v120 offset:128\n"
    "ds_read_b128 v[140:143], v120 offset:192\n"
    "ds_read_b128 v[144:147], v120 offset:256\n"
    "ds_read_b128 v[148:151], v120 offset:320\n"
    "ds_read_b128 v[152:155], v120 offset:384\n"
    "ds_read_b128 v[156:159], v120 offset:448\n"
    "ds_read_b128 v[160:163], v120 offset:512\n"
    "ds_read_b128 v[164:167], v120 offset:576\n"
    "ds_read_b128 v[168:171], v120 offset:640\n"
    "ds_read_b128 v[172:175], v120 offset:704\n"
    "ds_read_b128 v[176:179], v120 offset:768\n"
    "ds_read_b128 v[180:183], v120 offset:832\n"
    "s_waitcnt vmcnt(0) lgkmcnt(0)\n"
    "v_dot2c_f32_bf16 v122, v8, v128\n"
    "v_dot2c_f32_bf16 v123, v9, v129\n"
    "v_dot2c_f32_bf16 v122, v10, v130\n"
    "v_dot2c_f32_bf16 v123, v11, v131\n"
    "v_dot2c_f32_bf16 v122, v12, v132\n"
    "v_dot2c_f32_bf16 v123, v13, v133\n"
    "v_dot2c_f32_bf16 v122, v14, v134\n"
    "v_dot2c_f32_bf16 v123, v15, v135\n"
    "v_dot2c_f32_bf16 v122, v16, v136\n"
    "v_dot2c_f32_bf16 v123, v17, v137\n"
    "v_dot2c_f32_bf16 v122, v18, v138\n"
    "v_dot2c_f32_bf16 v123, v19, v139\n"
    "v_dot2c_f32_bf16 v122, v20, v140\n"
    "v_dot2c_f32_bf16 v123, v21, v141\n"
    "v_dot2c_f32_bf16 v122, v22, v142\n"
    "v_dot2c_f32_bf16 v123, v23, v143\n"
    "v_dot2c_f32_bf16 v122, v24, v144\n"
    "v_dot2c_f32_bf16 v123, v25, v145\n"
    "v_dot2c_f32_bf16 v122, v26, v146\n"
    "v_dot2c_f32_bf16 v123, v27, v147\n"
    "v_dot2c_f32_bf16 v122, v28, v148\n"
    "v_dot2c_f32_bf16 v123, v29, v149\n"
    "v_dot2c_f32_bf16 v122, v30, v150\n"
    "v_dot2c_f32_bf16 v123, v31, v151\n"
    "v_dot2c_f32_bf16 v122, v32, v152\n"
    "v_dot2c_f32_bf16 v123, v33, v153\n"
    "v_dot2c_f32_bf16 v122, v34, v154\n"
    "v_dot2c_f32_bf16 v123, v35, v155\n"
    "v_dot2c_f32_bf16 v122, v36, v156\n"
    "v_dot2c_f32_bf16 v123, v37, v157\n"
    "v_dot2c_f32_bf16 v122, v38, v158\n"
    "v_dot2c_f32_bf16 v123, v39, v159\n"
    "v_dot2c_f32_bf16 v122, v40, v160\n"
    "v_dot2c_f32_bf16 v123, v41, v161\n"
    "v_dot2c_f32_bf16 v122, v42, v162\n"
    "v_dot2c_f32_bf16 v123, v43, v163\n"
    "v_dot2c_f32_bf16 v122, v44, v164\n"
    "v_dot2c_f32_bf16 v123, v45, v165\n"
    "v_dot2c_f32_bf16 v122, v46, v166\n"
    "v_dot2c_f32_bf16 v123, v47, v167\n"
    "v_dot2c_f32_bf16 v122, v48, v168\n"
    "v_dot2c_f32_bf16 v123, v49, v169\n"
    "v_dot2c_f32_bf16 v122, v50, v170\n"
    "v_dot2c_f32_bf16 v123, v51, v171\n"
    "v_dot2c_f32_bf16 v122, v52, v172\n"
    "v_dot2c_f32_bf16 v123, v53, v173\n"
    "v_dot2c_f32_bf16 v122, v54, v174\n"
    "v_dot2c_f32_bf16 v123, v55, v175\n"
    "v_dot2c_f32_bf16 v122, v56, v176\n"
    "v_dot2c_f32_bf16 v123, v57, v177\n"
    "v_dot2c_f32_bf16 v122, v58, v178\n"
    "v_dot2c_f32_bf16 v123, v59, v179\n"
    "v_dot2c_f32_bf16 v122, v60, v180\n"
    "v_dot2c_f32_bf16 v123, v61, v181\n"
    "v_dot2c_f32_bf16 v122, v62, v182\n"
    "v_dot2c_f32_bf16 v123, v63, v183\n"
    "s_nop 2\n"
    "v_add_f32 v122, v122, v123\n"
    "s_nop 1\n"
    "v_add_f32_dpp v122, v122, v122 quad_perm:[1,0,3,2] row_mask:0xf bank_mask:0xf\n"
    "s_nop 1\n"
    "v_add_f32_dpp v122, v122, v122 quad_perm:[2,3,0,1] row_mask:0xf bank_mask:0xf\n"
    "v_mul_f32 v122, 0xbfb8aa3b, v122\n"
    "v_exp_f32 v122, v122\n"
    "v_and_b32 v120, 60, v255\n"
    "v_add_f32 v122, 1.0, v122\n"
    "v_rcp_f32 v122, v122\n"
    "s_nop 0\n"
    "buffer_store_dword v122, v120, s[24:27], s84 offen sc1\n"
    "s_waitcnt vmcnt(0)\n"
    "s_mov_b64 exec, 1\n"
    "ds_add_rtn_u32 v20, v249, v248 offset:4\n"
    "s_waitcnt lgkmcnt(0)\n"
    "v_readfirstlane_b32 s91, v20\n"
    "s_cmp_eq_u32 s91, 3\n"
    "s_cbranch_scc0 .Lgm_arr_68\n"
    "buffer_store_dwordx2 v[250:251], off, s[24:27], s73 sc1\n"
    "s_waitcnt vmcnt(0)\n"
    ".Lgm_arr_68:\n"
    "s_mov_b64 exec, -1\n"
    "s_waitcnt vmcnt(0)\n"
    "s_lshl_b32 s84, s2, 3\n"
    "s_add_u32 s84, s84, s44\n"
    "s_cmp_eq_u32 s84, 2047\n"
    "s_cbranch_scc0 .Lgm_nobump_64\n"
    "v_lshlrev_b32 v25, 7, v255\n"
    "s_sleep 64\n"
    "s_mov_b32 s89, 32896\n"
    "s_mov_b32 s91, 65536\n"
    ".Lgm_poll_69:\n"
    "buffer_load_dwordx2 v[26:27], v25, s[24:27], s89 offen sc1\n"
    "s_waitcnt vmcnt(0)\n"
    "v_cmp_ne_u64 vcc, s[74:75], v[26:27]\n"
    "s_and_b64 vcc, exec, vcc\n"
    "s_cbranch_scc0 .Lgm_polld_70\n"
    "s_sleep 32\n"
    "s_sub_u32 s91, s91, 1\n"
    "s_cbranch_scc0 .Lgm_poll_69\n"
    ".Lgm_polld_70:\n"
    "s_mov_b64 exec, -1\n"
    "s_mov_b32 s89, 41088\n"
    "s_mov_b32 s91, 65536\n"
    ".Lgm_poll_71:\n"
    "buffer_load_dwordx2 v[26:27], v25, s[24:27], s89 offen sc1\n"
    "s_waitcnt vmcnt(0)\n"
    "v_cmp_ne_u64 vcc, s[74:75], v[26:27]\n"
    "s_and_b64 vcc, exec, vcc\n"
    "s_cbranch_scc0 .Lgm_polld_72\n"
    "s_sleep 32\n"
    "s_sub_u32 s91, s91, 1\n"
    "s_cbranch_scc0 .Lgm_poll_71\n"
    ".Lgm_polld_72:\n"
    "s_mov_b64 exec, -1\n"
    "s_mov_b32 s89, 49280\n"
    "s_mov_b32 s91, 65536\n"
    ".Lgm_poll_73:\n"
    "buffer_load_dwordx2 v[26:27], v25, s[24:27], s89 offen sc1\n"
    "s_waitcnt vmcnt(0)\n"
    "v_cmp_ne_u64 vcc, s[74:75], v[26:27]\n"
    "s_and_b64 vcc, exec, vcc\n"
    "s_cbranch_scc0 .Lgm_polld_74\n"
    "s_sleep 32\n"
    "s_sub_u32 s91, s91, 1\n"
    "s_cbranch_scc0 .Lgm_poll_73\n"
    ".Lgm_polld_74:\n"
    "s_mov_b64 exec, -1\n"
    "s_mov_b32 s89, 57472\n"
    "s_mov_b32 s91, 65536\n"
    ".Lgm_poll_75:\n"
    "buffer_load_dwordx2 v[26:27], v25, s[24:27], s89 offen sc1\n"
    "s_waitcnt vmcnt(0)\n"
    "v_cmp_ne_u64 vcc, s[74:75], v[26:27]\n"
    "s_and_b64 vcc, exec, vcc\n"
    "s_cbranch_scc0 .Lgm_polld_76\n"
    "s_sleep 32\n"
    "s_sub_u32 s91, s91, 1\n"
    "s_cbranch_scc0 .Lgm_poll_75\n"
    ".Lgm_polld_76:\n"
    "s_mov_b64 exec, -1\n"
    "s_mov_b64 exec, 1\n"
    "buffer_store_dwordx2 v[250:251], off, s[24:27], 0 sc1\n"
    "s_mov_b64 exec, -1\n"
    ".Lgm_nobump_64:\n"
    "s_endpgm\n"
    ".Lgm_lc_22:\n"
    "s_mov_b32 s90, 1048576\n"
    ".Lgm_lw_77:\n"
    "ds_read_b32 v247, v246 offset:48\n"
    "s_waitcnt lgkmcnt(0)\n"
    "v_readfirstlane_b32 s91, v247\n"
    "s_cmp_ge_u32 s91, 2\n"
    "s_cbranch_scc1 .Lgm_lwd_78\n"
    "s_sub_u32 s90, s90, 1\n"
    "s_cbranch_scc0 .Lgm_lw_77\n"
    ".Lgm_lwd_78:\n"
    "s_branch .Lgm_lcb_23\n"
    ".Lgm_lc_39:\n"
    "s_mov_b32 s90, 1048576\n"
    ".Lgm_lw_79:\n"
    "ds_read_b32 v127, v249 offset:12\n"
    "s_waitcnt lgkmcnt(0)\n"
    "v_readfirstlane_b32 s91, v127\n"
    "s_cmp_ge_u32 s91, 8\n"
    "s_cbranch_scc1 .Lgm_lwd_80\n"
    "s_sub_u32 s90, s90, 1\n"
    "s_cbranch_scc0 .Lgm_lw_79\n"
    ".Lgm_lwd_80:\n"
    "s_branch .Lgm_lcb_40\n"
    ".Lgm_a_noc_45:\n"
    "s_waitcnt vmcnt(0)\n"
    "s_mov_b64 exec, 1\n"
    "ds_add_rtn_u32 v20, v249, v248 offset:8\n"
    "s_waitcnt lgkmcnt(0)\n"
    "v_readfirstlane_b32 s91, v20\n"
    "s_cmp_eq_u32 s91, 3\n"
    "s_cbranch_scc0 .Lgm_arr_81\n"
    "buffer_store_dwordx2 v[250:251], off, s[24:27], s64 sc1\n"
    ".Lgm_arr_81:\n"
    "s_mov_b64 exec, -1\n"
    "s_endpgm\n"
    ".Lgm_slow_43:\n"
    "v_mov_b32 v16, 0xff800000\n"
    "s_mov_b32 s71, 262144\n"
    ".Lgm_slow1_82:\n"
    "buffer_load_dwordx4 v[0:3], v106, s[24:27], s71 offen sc1\n"
    "buffer_load_dwordx4 v[4:7], v106, s[24:27], s71 offen offset:1024 sc1\n"
    "s_waitcnt vmcnt(0)\n"
    "v_mfma_f32_32x32x16_bf16 v[64:79], v[0:3], v[216:219], 0\n"
    "v_mfma_f32_32x32x16_bf16 v[64:79], v[4:7], v[220:223], v[64:79]\n"
    "s_nop 11\n"
    "v_max3_f32 v16, v16, v64, v65\n"
    "v_max3_f32 v16, v16, v66, v67\n"
    "v_max3_f32 v16, v16, v68, v69\n"
    "v_max3_f32 v16, v16, v70, v71\n"
    "v_max3_f32 v16, v16, v72, v73\n"
    "v_max3_f32 v16, v16, v74, v75\n"
    "v_max3_f32 v16, v16, v76, v77\n"
    "v_max3_f32 v16, v16, v78, v79\n"
    "s_add_u32 s71, s71, 86016\n"
    "s_cmp_lt_u32 s71, 5767168\n"
    "s_cbranch_scc1 .Lgm_slow1_82\n"
    "v_mov_b32 v17, v16\n"
    "s_nop 1\n"
    "v_permlane32_swap_b32 v16, v17\n"
    "v_max_f32 v16, v16, v17\n"
    "s_mov_b32 s71, 262144\n"
    "v_mov_b32 v8, v125\n"
    "v_mov_b32 v9, v125\n"
    "v_mov_b32 v10, v125\n"
    "v_mov_b32 v11, v125\n"
    "v_mov_b32 v12, v125\n"
    "v_mov_b32 v13, v125\n"
    "v_mov_b32 v14, v125\n"
    "v_mov_b32 v15, v125\n"
    "v_mov_b32 v176, 0\n"
    "v_mov_b32 v177, 0\n"
    "v_mov_b32 v178, 0\n"
    "v_mov_b32 v179, 0\n"
    "v_mov_b32 v180, 0\n"
    "v_mov_b32 v181, 0\n"
    "v_mov_b32 v182, 0\n"
    "v_mov_b32 v183, 0\n"
    "v_mov_b32 v184, 0\n"
    "v_mov_b32 v185, 0\n"
    "v_mov_b32 v186, 0\n"
    "v_mov_b32 v187, 0\n"
    "v_mov_b32 v188, 0\n"
    "v_mov_b32 v189, 0\n"
    "v_mov_b32 v190, 0\n"
    "v_mov_b32 v191, 0\n"
    ".Lgm_slow2_83:\n"
    "buffer_load_dwordx4 v[0:3], v106, s[24:27], s71 offen sc1\n"
    "buffer_load_dwordx4 v[4:7], v106, s[24:27], s71 offen offset:1024 sc1\n"
    "s_mov_b64 exec, s[80:81]\n"
    "buffer_load_dwordx4 v[8:11], v107, s[24:27], s71 offen sc1\n"
    "buffer_load_dwordx4 v[12:15], v108, s[24:27], s71 offen sc1\n"
    "s_mov_b64 exec, -1\n"
    "s_waitcnt vmcnt(0)\n"
    "v_mfma_f32_32x32x16_bf16 v[64:79], v[0:3], v[216:219], 0\n"
    "v_mfma_f32_32x32x16_bf16 v[64:79], v[4:7], v[220:223], v[64:79]\n"
    "s_nop 11\n"
    "v_sub_f32 v64, v64, v16\n"
    "v_sub_f32 v65, v65, v16\n"
    "v_sub_f32 v66, v66, v16\n"
    "v_sub_f32 v67, v67, v16\n"
    "v_sub_f32 v68, v68, v16\n"
    "v_sub_f32 v69, v69, v16\n"
    "v_sub_f32 v70, v70, v16\n"
    "v_sub_f32 v71, v71, v16\n"
    "v_sub_f32 v72, v72, v16\n"
    "v_sub_f32 v73, v73, v16\n"
    "v_sub_f32 v74, v74, v16\n"
    "v_sub_f32 v75, v75, v16\n"
    "v_sub_f32 v76, v76, v16\n"
    "v_sub_f32 v77, v77, v16\n"
    "v_sub_f32 v78, v78, v16\n"
    "v_sub_f32 v79, v79, v16\n"
    "v_exp_f32 v64, v64\n"
    "v_exp_f32 v65, v65\n"
    "v_exp_f32 v66, v66\n"
    "v_exp_f32 v67, v67\n"
    "v_exp_f32 v68, v68\n"
    "v_exp_f32 v69, v69\n"
    "v_exp_f32 v70, v70\n"
    "v_exp_f32 v71, v71\n"
    "v_exp_f32 v72, v72\n"
    "v_exp_f32 v73, v73\n"
    "v_exp_f32 v74, v74\n"
    "v_exp_f32 v75, v75\n"
    "v_exp_f32 v76, v76\n"
    "v_exp_f32 v77, v77\n"
    "v_exp_f32 v78, v78\n"
    "v_exp_f32 v79, v79\n"
    "v_smfmac_f32_32x32x32_bf16 v[176:191], v[8:11], v[64:71], v104\n"
    "s_nop 0\n"
    "v_smfmac_f32_32x32x32_bf16 v[176:191], v[12:15], v[72:79], v104\n"
    "s_add_u32 s71, s71, 86016\n"
    "s_cmp_lt_u32 s71, 5767168\n"
    "s_cbranch_scc1 .Lgm_slow2_83\n"
    "s_nop 11\n"
    "v_mov_b32 v60, v188\n"
    "v_mov_b32 v61, v188\n"
    "s_nop 1\n"
    "v_permlane32_swap_b32 v60, v61\n"
    "v_rcp_f32 v62, v61\n"
    "s_branch .Lgm_norm_44\n"
    ".Lgm_end_3:\n"
    : "+{v0}"(tid)
    : "{s[0:1]}"(ka), "{s2}"(bid), "{s3}"((unsigned)(unsigned long long)(__attribute__((address_space(3))) char*)smem)
    : "v1", "v2", "v3", "v4", "v5", "v6", "v7", "v8", "v9", "v10", "v11", "v12", "v13", "v14", "v15", "v16", "v17", "v18", "v19", "v20", "v21", "v22", "v23", "v24", "v25", "v26", "v27", "v28", "v29", "v30", "v31", "v32", "v33", "v34", "v35", "v36", "v37", "v38", "v39", "v40", "v41", "v42", "v43", "v44", "v45", "v46", "v47", "v48", "v49", "v50", "v51", "v52", "v53", "v54", "v55", "v56", "v57", "v58", "v59", "v60", "v61", "v62", "v63", "v64", "v65", "v66", "v67", "v68", "v69", "v70", "v71", "v72", "v73", "v74", "v75", "v76", "v77", "v78", "v79", "v80", "v81", "v82", "v83", "v84", "v85", "v86", "v87", "v88", "v89", "v90", "v91", "v92", "v93", "v94", "v95", "v96", "v97", "v98", "v99", "v100", "v101", "v102", "v103", "v104", "v105", "v106", "v107", "v108", "v109", "v110", "v111", "v112", "v113", "v114", "v115", "v116", "v117", "v118", "v119", "v120", "v121", "v122", "v123", "v124", "v125", "v126", "v127", "v128", "v129", "v130", "v131", "v132", "v133", "v134", "v135", "v136", "v137", "v138", "v139", "v140", "v141", "v142", "v143", "v144", "v145", "v146", "v147", "v148", "v149", "v150", "v151", "v152", "v153", "v154", "v155", "v156", "v157", "v158", "v159", "v160", "v161", "v162", "v163", "v164", "v165", "v166", "v167", "v168", "v169", "v170", "v171", "v172", "v173", "v174", "v175", "v176", "v177", "v178", "v179", "v180", "v181", "v182", "v183", "v184", "v185", "v186", "v187", "v188", "v189", "v190", "v191", "v192", "v193", "v194", "v195", "v196", "v197", "v198", "v199", "v200", "v201", "v202", "v203", "v204", "v205", "v206", "v207", "v208", "v209", "v210", "v211", "v212", "v213", "v214", "v215", "v216", "v217", "v218", "v219", "v220", "v221", "v222", "v223", "v224", "v225", "v226", "v227", "v228", "v229", "v230", "v231", "v232", "v233", "v234", "v235", "v236", "v237", "v238", "v239", "v240", "v241", "v242", "v243", "v244", "v245", "v246", "v247", "v248", "v249", "v250", "v251", "v252", "v253", "v254", "v255", "s4", "s5", "s6", "s7", "s8", "s9", "s10", "s11", "s12", "s13", "s14", "s15", "s16", "s17", "s18", "s19", "s20", "s21", "s22", "s23", "s24", "s25", "s26", "s27", "s28", "s29", "s30", "s31", "s33", "s34", "s35", "s36", "s37", "s38", "s39", "s40", "s41", "s42", "s43", "s44", "s45", "s46", "s47", "s48", "s49", "s50", "s51", "s52", "s53", "s54", "s55", "s56", "s57", "s58", "s59", "s60", "s61", "s62", "s63", "s64", "s65", "s66", "s67", "s68", "s69", "s70", "s71", "s72", "s73", "s74", "s75", "s76", "s77", "s78", "s79", "s80", "s81", "s82", "s83", "s84", "s85", "s86", "s87", "s88", "s89", "s90", "s91", "s92", "s93", "s94", "s95", "s96", "s97", "s98", "s99", "vcc", "scc", "memory");
#endif
}

extern "C" void kernel_launch(void* const* d_in, const int* in_sizes, int n_in, void* d_out, int out_size, void* d_ws, size_t ws_size,
                              hipStream_t stream) {
    const unsigned F = 0x00020000u;
    const Args a = {{d_in[0], 2048u * 896u, F}, {d_in[1], 448u * 2688u, F}, {d_in[4], 448u * 3584u, F}, {d_in[5], 3584u, F}, {(char*)d_ws + (0), 15204352u, F},
                    {d_in[6], 1792u * 896u, F}, {d_in[2], 448u * 896u, F}, {d_in[7], 896u, F}, {d_out, 2048u * 896u, F}};
    gm_fused<<<256, 512, 0, stream>>>(a);
}
```
